```python
import torch

def _softmax(x, axis=-1):
    return torch.softmax(x, dim=axis)
def _gelu(x):
    return torch.nn.functional.gelu(x, approximate="tanh")
def _silu(x):
    return torch.nn.functional.silu(x)
def _sigmoid(x):
    return torch.sigmoid(x)
def _layernorm(x, axis=-1, eps=1e-5):
    m = x.mean(dim=axis, keepdim=True)
    v = x.var(dim=axis, keepdim=True, unbiased=False)
    return (x - m) / torch.sqrt(v + eps)
def _scatter_add(tgt, idx, src, axis=0):
    return tgt.clone().index_add_(axis, idx, src)
def _segment_sum(src, seg, n):
    out = torch.zeros((n,) + src.shape[1:], dtype=src.dtype)
    return out.index_add_(0, seg, src)
def _pad(x, pw):
    flat = tuple(v for pair in reversed(pw) for v in pair)
    return torch.nn.functional.pad(x, flat)

kernel_name = 'concat_heads__ssm_conv_s4d__spectral_gfnet__prompt'

def setup_inputs():

    _w = (lambda a: (lambda t: t.to(torch.bfloat16) if t.is_floating_point() else t)(torch.as_tensor(a)))
    def _A_setup():
        Bsz, L, D, N = 1, 2048, 576, 128
        return {
            "u": _w((lambda *s: torch.randn(*s, dtype=torch.bfloat16) * 0.05)(Bsz, L, D)),
            "log_A_real": _w((lambda *s: torch.randn(*s, dtype=torch.bfloat16) * 6.4)(D, N) * 0.5),
            "A_imag": _w((lambda *s: torch.randn(*s, dtype=torch.bfloat16) * 0.05)(D, N) * 3.14),
            "C_real": _w((lambda *s: torch.randn(*s, dtype=torch.bfloat16) * 0.05)(D, N) / N ** 0.5),
            "C_imag": _w((lambda *s: torch.randn(*s, dtype=torch.bfloat16) * 0.05)(D, N) / N ** 0.5),
            "log_dt": _w((lambda *s: torch.randn(*s, dtype=torch.bfloat16) * 0.05)(D) * 0.5 - 4.0),
            "D_skip": _w((lambda *s: torch.randn(*s, dtype=torch.bfloat16) * 0.05)(D)),
        }
    _A_in = _A_setup()

    def _B_setup():
        B, S, D = 1, 2048, 576
        Df = D // 2 + 1
        return {
            "x": _w((lambda *s: torch.randn(*s, dtype=torch.bfloat16) * 0.05)(B, S, D)),
            "w_r": _w((lambda *s: torch.randn(*s, dtype=torch.bfloat16) * 0.8)(S, Df) * 0.02),
            "w_i": _w((lambda *s: torch.randn(*s, dtype=torch.bfloat16) * 0.8)(S, Df) * 0.02),
        }
    _B_in = _B_setup()

    return {
        "A_u": _A_in["u"],
        "A_log_A_real": _A_in["log_A_real"],
        "A_A_imag": _A_in["A_imag"],
        "A_C_real": _A_in["C_real"],
        "A_C_imag": _A_in["C_imag"],
        "A_log_dt": _A_in["log_dt"],
        "A_D_skip": _A_in["D_skip"],
        "B_x": _B_in["x"],
        "B_w_r": _B_in["w_r"],
        "B_w_i": _B_in["w_i"],
    }

def reference(A_u, A_log_A_real, A_A_imag, A_C_real, A_C_imag, A_log_dt, A_D_skip, B_x, B_w_r, B_w_i):
    def _A_ref(u, log_A_real, A_imag, C_real, C_imag, log_dt, D_skip):
        L = u.shape[1]
        dt = torch.exp(log_dt)
        A = -torch.exp(log_A_real) + 1j * A_imag
        C = (C_real + 1j * C_imag) * dt[:, None]
        pos = (lambda x, dt: x.to(dt))(torch.arange(L), log_dt.dtype)
        P = torch.exp((A * dt[:, None])[:, :, None] * pos[None, None, :])
        K = 2.0 * torch.einsum("dn,dnl->dl", C, P).real
        L2 = 2 * L
        u_t = (lambda x, *d: x.permute(*d))(u, 0, 2, 1)
        Kf = (lambda x, *a, **k: torch.fft.rfft(x.float(), *a, **k))(K, L2)
        Uf = (lambda x, *a, **k: torch.fft.rfft(x.float(), *a, **k))(u_t, L2)
        y = (lambda x, *a, **k: torch.fft.irfft(x, *a, **k).to(torch.bfloat16))(Uf * Kf[None, :, :], L2)[:, :, :L]
        y = (lambda x, *d: x.permute(*d))(y, 0, 2, 1)
        return y + u * D_skip[None, None, :]

    def _B_ref(x, w_r, w_i):
        B, S, D = x.shape
        X = (lambda x, axes=(-2,-1): torch.fft.rfft2(x.float(), dim=axes))(x, axes=(-2, -1))
        W = w_r + 1j * w_i
        X = X * W
        y = (lambda x, s, axes=(-2,-1): torch.fft.irfft2(x, s=s, dim=axes).to(torch.bfloat16))(X, (S, D), axes=(-2, -1))
        return y

    a = _A_ref(A_u, A_log_A_real, A_A_imag, A_C_real, A_C_imag, A_log_dt, A_D_skip)
    b = _B_ref(B_x, B_w_r, B_w_i)
    return torch.concatenate([a, b], axis=-1)
```

```cpp
#include <hip/hip_runtime.h>
#include <hip/hip_bf16.h>
#include <stdint.h>

#define GAS __attribute__((address_space(1)))
#define LAS __attribute__((address_space(3)))
#define DI __device__ __forceinline__
typedef __bf16 bf16x8 __attribute__((ext_vector_type(8)));
typedef __bf16 bf16x2 __attribute__((ext_vector_type(2)));
typedef float f32x2 __attribute__((ext_vector_type(2)));
typedef float f32x4 __attribute__((ext_vector_type(4)));
typedef float f32x16 __attribute__((ext_vector_type(16)));
typedef unsigned u32x2 __attribute__((ext_vector_type(2)));
typedef unsigned u32x4 __attribute__((ext_vector_type(4)));
typedef short s16x4 __attribute__((ext_vector_type(4)));
typedef unsigned short u16;
typedef __amdgpu_buffer_rsrc_t rsrc_t;

DI unsigned pk(float a, float b) { f32x2 v = {a, b}; return __builtin_bit_cast(unsigned, __builtin_convertvector(v, bf16x2)); }
DI float bflo(unsigned v) { return __builtin_bit_cast(float, v << 16); }
DI float bfhi(unsigned v) { return __builtin_bit_cast(float, v & 0xffff0000u); }
DI float bfround(float x) { return bflo(pk(x, x)); }
DI f32x4 mfma16(u32x4 a, u32x4 b, f32x4 c) { return __builtin_amdgcn_mfma_f32_16x16x32_bf16(__builtin_bit_cast(bf16x8, a), __builtin_bit_cast(bf16x8, b), c, 0, 0, 0); }
DI f32x16 mfma32(u32x4 a, u32x4 b, f32x16 c) { return __builtin_amdgcn_mfma_f32_32x32x16_bf16(__builtin_bit_cast(bf16x8, a), __builtin_bit_cast(bf16x8, b), c, 0, 0, 0); }
DI float cosr(float x) { return __builtin_amdgcn_cosf(__builtin_amdgcn_fractf(x)); }
DI float sinr(float x) { return __builtin_amdgcn_sinf(__builtin_amdgcn_fractf(x)); }
DI u32x2 trread(LAS char* p) { return __builtin_bit_cast(u32x2, __builtin_amdgcn_ds_read_tr16_b64_v4i16((LAS s16x4*)p)); }
template <class T> DI T ldsr(LAS char* p) { return *(LAS T*)p; }
template <class T> DI void ldsw(LAS char* p, T v) { *(LAS T*)p = v; }
DI int rfl(int x) { return __builtin_amdgcn_readfirstlane(x); }

struct Params {
    const u16 *u, *logA, *Aim, *Cre, *Cim, *logdt, *Dskip, *x, *wr, *wi;
    u16* out; char* ws;
};

constexpr unsigned ROWDW = 640;
constexpr unsigned E1OFF = 0, E2OFF = 4u << 20, UTOFF = 8u << 20, YTOFF = 12u << 20, WSBYTES = 20u << 20;
constexpr int B_XT = 0, B_SLAB = 0, SLABDW = 644, B_ZI = 41472, IMG = 676;
constexpr int UCOPY = 4160, UCH = 4 * UCOPY, KREVB = 4576, CHB = UCH + KREVB;
constexpr int A_DUMP = 3 * CHB;
constexpr int A_TAB = 0, TABB = 53 * 68 * 4, A_KP = 4 * TABB;
constexpr int LDS_BYTES = 81920;

DI rsrc_t ws_rsrc(const Params& P) { return __builtin_amdgcn_make_buffer_rsrc(P.ws, 0, WSBYTES, 0x00020000); }

struct DftC { u32x4 P1[3], Q1[3], P2[3], Q2[3]; float tc[12], ts[12]; };

DI void dft24_frags(int col, int h, u32x4 (&Pf)[3], u32x4 (&Qf)[3]) {
#pragma unroll
    for (int s = 0; s < 3; ++s)
#pragma unroll
        for (int i = 0; i < 4; ++i) {
            const float ang = (float)((8 * s + 4 * h + i) * col) * (1.0f / 24.0f);
            const float c = cosr(ang), sn = sinr(ang);
            Pf[s][i] = pk(c, sn); Qf[s][i] = pk(-sn, c);
        }
}
DI void dft576_consts(DftC& C, int l, bool special, int s2, float scale) {
    const int n = l & 31, h = l >> 5;
    dft24_frags(n, h, C.P2, C.Q2);
    int ka = n; float extra = 0.f;
    if (special) {
        const bool hi = n < 16 ? n >= 8 : n < 24 ? n >= 20 : (n & 1);
        ka = n < 16 ? (n & 7) : n < 24 ? 8 + (n & 3) : 12; extra = hi ? (float)s2 * (1.0f / 128.0f) : 0.f;
        u32x4 Pt[3], Qt[3]; dft24_frags(ka, h, Pt, Qt);
#pragma unroll
        for (int s = 0; s < 3; ++s)
#pragma unroll
            for (int i = 0; i < 4; ++i) {
                C.P1[s][i] = hi ? (Qt[s][i] & 0xffff0000u) : (Pt[s][i] & 0xffffu);
                C.Q1[s][i] = hi ? (Qt[s][i] << 16) : (Qt[s][i] & 0xffffu);
            }
    } else {
#pragma unroll
        for (int s = 0; s < 3; ++s) { C.P1[s] = C.P2[s]; C.Q1[s] = C.Q2[s]; }
    }
#pragma unroll
    for (int r = 0; r < 12; ++r) {
        const int b = (r & 3) + 8 * (r >> 2) + 4 * h;
        const float ang = (float)(b * ka) * (1.0f / 576.0f) + extra;
        C.tc[r] = cosr(ang) * scale; C.ts[r] = sinr(ang) * scale;
    }
}
DI void dft576(LAS char* img, int l, const DftC& C, f32x16& oR, f32x16& oI) {
    const int n = l & 31, h = l >> 5;
    LAS char* p = img + ((n < 24 ? n : 23) * 28 + 4 * h) * 4;
    f32x16 aR = {}, aI = {};
#pragma unroll
    for (int s = 0; s < 3; ++s) { const u32x4 z = ldsr<u32x4>(p + 32 * s); aR = mfma32(z, C.P1[s], aR); aI = mfma32(z, C.Q1[s], aI); }
    oR = (f32x16){}; oI = (f32x16){};
#pragma unroll
    for (int s = 0; s < 3; ++s) {
        u32x4 z;
#pragma unroll
        for (int i = 0; i < 4; ++i) { const int r = 4 * s + i;
            z[i] = pk(aR[r] * C.tc[r] + aI[r] * C.ts[r], aI[r] * C.tc[r] - aR[r] * C.ts[r]); }
        oR = mfma32(z, C.P2[s], oR); oI = mfma32(z, C.Q2[s], oI);
    }
}

template <int AUX> DI void phase_p1(const Params& P, int b, LAS char* lds) {
    const int tid = threadIdx.x, l = tid & 63, w = rfl(tid >> 6);
    const int s2 = (b & 7) * 8 + ((b >> 3) & 7), j = b >> 6;
#pragma unroll
    for (int i = 0; i < 9; ++i) {
        const int n = (i * 4 + w) * 64 + l, r = n / 72, pp = n - 72 * r, p = pp ^ (((r >> 1) & 3) << 1);
        __builtin_amdgcn_global_load_lds((const GAS void*)((const GAS char*)P.x + (size_t)((64 * r + s2) * 1152 + p * 16)), (LAS void*)(lds + B_XT + (i * 4 + w) * 1024), 16, 0, 0);
    }
    const int c = l & 15, g = l >> 4;
    u32x4 M1;
    {
        const int i = c & 3, kap = 4 * j + (c >> 2);
        float v[8];
#pragma unroll
        for (int q = 0; q < 8; ++q) {
            const int s1 = (q < 4 ? 0 : 12) + 4 * g + q;
            float ang = (float)(((64 * s1 + s2) * kap) & 2047) * (1.0f / 2048.0f) + (i == 1 ? 0.25f : 0.f);
            if (kap == 0 && i == 1) ang = (q & 1) ? 0.5f : 0.f;
            v[q] = i < 2 ? cosr(ang) : 0.f;
        }
#pragma unroll
        for (int q = 0; q < 4; ++q) M1[q] = pk(v[2 * q], v[2 * q + 1]);
    }
    const bool special = (j == 0 && w == 0);
    DftC C; dft576_consts(C, l, special, s2, 1.0f);
    const int q = c >> 2, pp = c & 3;
    LAS char* xa = lds + B_XT + ((4 * g + q) * 72 + 2 * (w ^ ((2 * g + (q >> 1)) & 3)) + (pp >> 1)) * 16 + 8 * (pp & 1);
    LAS char* za[3];
#pragma unroll
    for (int m = 0; m < 3; ++m) { const int d = 64 * m + 16 * w + c, a = d / 24, bb = d - 24 * a; za[m] = lds + B_ZI + (g * IMG + bb * 28 + a) * 4; }
    __syncthreads();
#pragma unroll
    for (int m = 0; m < 9; ++m) {
        const u32x2 lo = trread(xa + 128 * m), hi = trread(xa + 128 * m + 16 * 1152);
        const f32x4 d = mfma16(M1, (u32x4){lo[0], lo[1], hi[0], hi[1]}, (f32x4){});
        ldsw<unsigned>(za[m % 3] + 32 * (m / 3), pk(d[0], d[1]));
    }
    __syncthreads();
    f32x16 oR, oI;
    dft576(lds + B_ZI + w * IMG * 4, l, C, oR, oI);
    const rsrc_t rs = ws_rsrc(P);
    const int kb = l & 31, h = l >> 5;
    const unsigned rowoff = E1OFF + (unsigned)((4 * j + w) * 64 + s2) * (ROWDW * 4);
    if (kb < 24) {
#pragma unroll
        for (int i = 0; i < 3; ++i) {
            const u32x4 v = {pk(oR[4 * i], oI[4 * i]), pk(oR[4 * i + 1], oI[4 * i + 1]), pk(oR[4 * i + 2], oI[4 * i + 2]), pk(oR[4 * i + 3], oI[4 * i + 3])};
            const int pos = (i == 2 && special) ? 384 + 96 * h + 4 * kb : i * 192 + kb * 8 + h * 4;
            __builtin_amdgcn_raw_buffer_store_b128(v, rs, rowoff + (unsigned)pos * 4, 0, AUX);
        }
    }
    if (special && h == 0) {
        __builtin_amdgcn_raw_buffer_store_b32(pk(oR[12], oI[12]), rs, rowoff + (unsigned)(576 + kb) * 4, 0, AUX);
        __builtin_amdgcn_raw_buffer_store_b32(pk(oR[13], oI[13]), rs, rowoff + (unsigned)(608 + kb) * 4, 0, AUX);
    }
}

DI void radix4(f32x4 (&R)[4], f32x4 (&I)[4]) {
    const f32x4 aR = R[0] + R[2], aI = I[0] + I[2], bR = R[0] - R[2], bI = I[0] - I[2];
    const f32x4 cR = R[1] + R[3], cI = I[1] + I[3], dR = R[1] - R[3], dI = I[1] - I[3];
    R[0] = aR + cR; I[0] = aI + cI; R[2] = aR - cR; I[2] = aI - cI;
    R[1] = bR + dI; I[1] = bI - dR; R[3] = bR - dI; I[3] = bI + dR;
}
template <int AUX> DI void phase_p2(const Params& P, int y) {
    const int tid = threadIdx.x, l = tid & 63, w = rfl(tid >> 6);
    int row, pos0;
    if (y < 135) { row = 1 + y / 9; pos0 = 16 * (4 * (y % 9) + w); }
    else { row = 0; pos0 = 16 * (4 * (y - 135) + w); }
    if (y >= 145) return;
    const int c = l & 15, g = l >> 4;
    const int p = pos0 + c;
    int kap = row, ka, kb;
    if (row != 0) { const int i = pos0 / 192, rem = p - 192 * i; kb = rem >> 3; ka = 8 * i + (rem & 7); }
    else if (pos0 < 384) { kap = pos0 >= 192 ? 16 : 0; const int rem = p - (pos0 >= 192 ? 192 : 0); kb = rem >> 3; ka = rem & 7; }
    else if (pos0 < 576) { kap = pos0 >= 480 ? 16 : 0; const int rem = p - (pos0 >= 480 ? 480 : 384); kb = rem >> 2; ka = 8 + (rem & 3); }
    else { kap = pos0 >= 608 ? 16 : 0; kb = (p & 31) < 24 ? (p & 31) : (p & 31) - 24; ka = 12; }
    const int kd = ka + 24 * kb;
    const bool mir = kd > 288, edge = (kd == 0 || kd == 288);
    const int col = mir ? 576 - kd : kd;
    f32x4 Wr[4], Wi[4];
    {
        u16 a[16], bq[16];
#pragma unroll
        for (int t = 0; t < 16; ++t) {
            const int ks = kap + 32 * (16 * (t >> 2) + 4 * g + (t & 3));
            const int rr = mir ? ((2048 - ks) & 2047) : ks;
            a[t] = ((const GAS u16*)P.wr)[rr * 289 + col]; bq[t] = ((const GAS u16*)P.wi)[rr * 289 + col];
        }
#pragma unroll
        for (int t = 0; t < 16; ++t) { Wr[t >> 2][t & 3] = bflo(a[t]); const float v = bflo(bq[t]); Wi[t >> 2][t & 3] = mir ? -v : v; }
        if (__any(edge)) {
#pragma unroll
            for (int t = 0; t < 16; ++t) {
                const int ks = kap + 32 * (16 * (t >> 2) + 4 * g + (t & 3));
                const int rr = (2048 - ks) & 2047;
                a[t] = ((const GAS u16*)P.wr)[rr * 289 + col]; bq[t] = ((const GAS u16*)P.wi)[rr * 289 + col];
            }
#pragma unroll
            for (int t = 0; t < 16; ++t) if (edge) { Wr[t >> 2][t & 3] = 0.5f * (Wr[t >> 2][t & 3] + bflo(a[t])); Wi[t >> 2][t & 3] = 0.5f * (Wi[t >> 2][t & 3] - bflo(bq[t])); }
        }
    }
    u32x4 Pf[4], Qf[4], Pi[4], Qi[4];
#pragma unroll
    for (int t = 0; t < 4; ++t)
#pragma unroll
        for (int i = 0; i < 4; ++i) {
            const int uu = 4 * g + i;
            const float a1 = (float)((c * (t + 4 * uu)) & 63) * (1.0f / 64.0f), a2 = (float)((uu * (t + 4 * c)) & 63) * (1.0f / 64.0f) + (kap == 16 ? (float)(t + 4 * c) * (1.0f / 128.0f) : 0.f);
            const float c1 = cosr(a1), s1 = sinr(a1), c2 = cosr(a2), s2 = sinr(a2);
            Pf[t][i] = pk(c1, s1); Qf[t][i] = pk(-s1, c1); Pi[t][i] = pk(c2, s2); Qi[t][i] = pk(-s2, c2);
        }
    const rsrc_t rs = ws_rsrc(P);
    u32x4 X[4];
    {
        const unsigned base = E1OFF + (unsigned)(row * 64 + 16 * g) * (ROWDW * 4) + (unsigned)p * 4;
#pragma unroll
        for (int t = 0; t < 4; ++t)
#pragma unroll
            for (int i = 0; i < 4; ++i) X[t][i] = __builtin_amdgcn_raw_buffer_load_b32(rs, base, (t + 4 * i) * (ROWDW * 4), AUX);
    }
    f32x4 R[4], I[4];
#pragma unroll
    for (int t = 0; t < 4; ++t) { R[t] = mfma16(Pf[t], X[t], (f32x4){}); I[t] = mfma16(Qf[t], X[t], (f32x4){}); }
    radix4(R, I);
#pragma unroll
    for (int t = 0; t < 4; ++t) { const f32x4 zr = R[t] * Wr[t] - I[t] * Wi[t], zi = -(R[t] * Wi[t]) - I[t] * Wr[t]; R[t] = zr; I[t] = zi; }
    radix4(R, I);
#pragma unroll
    for (int t = 0; t < 4; ++t) {
        const u32x4 hq = {pk(R[t][0], I[t][0]), pk(R[t][1], I[t][1]), pk(R[t][2], I[t][2]), pk(R[t][3], I[t][3])};
        const f32x4 oR = mfma16(hq, Pi[t], (f32x4){}), oI = mfma16(hq, Qi[t], (f32x4){});
        const u32x4 v = {pk(oR[0], -oI[0]), pk(oR[1], -oI[1]), pk(oR[2], -oI[2]), pk(oR[3], -oI[3])};
        __builtin_amdgcn_raw_buffer_store_b128(v, rs, E2OFF + (unsigned)((t + 4 * c) * 16 + row) * (ROWDW * 4) + (unsigned)(pos0 + 4 * g) * 4, 0, AUX);
    }
}

DI int pos_norm(int ka, int kb) { return 192 * (ka >> 3) + 8 * kb + (ka & 7); }
DI int pos_spec(int cls, int ka, int kb) { return ka < 8 ? 192 * cls + 8 * kb + ka : ka < 12 ? 376 + 96 * cls + 4 * kb + ka : 576 + 32 * cls + kb; }
template <int AUX> DI void phase_p3(const Params& P, int b, LAS char* lds) {
    const int tid = threadIdx.x, l = tid & 63, w = rfl(tid >> 6);
    const int s2 = (b & 7) * 8 + ((b >> 3) & 7), j = b >> 6;
    const rsrc_t rs = ws_rsrc(P);
    u32x4 sl[10]; int dst[10];
#pragma unroll
    for (int m = 0; m < 10; ++m) {
        int r, ch;
        if (m < 9) { const int n = m * 256 + tid; r = n / 144; ch = n - 144 * r; } else { r = 0; ch = 144 + (tid & 15); }
        sl[m] = __builtin_amdgcn_raw_buffer_load_b128(rs, E2OFF + (unsigned)(s2 * 16 + r) * (ROWDW * 4) + (unsigned)ch * 16, 0, AUX);
        dst[m] = r * (SLABDW * 4) + ch * 16;
    }
    const int c = l & 15, g = l >> 4;
    u32x4 M3[2];
    {
        const int pr = c >> 2, o = c & 3, sa = 64 * (8 * j + 2 * pr) + s2, sb = sa + 64;
#pragma unroll
        for (int ks = 0; ks < 2; ++ks)
#pragma unroll
            for (int i = 0; i < 4; ++i) {
                const int m = 4 * g + i; const bool mirr = (ks == 1 && m != 0); const int kap = (ks == 1 && m == 0) ? 16 : m;
                const float aa = kap == 16 ? 0.f : (float)((sa * kap) & 2047) * (1.0f / 2048.0f), ab = kap == 16 ? 0.5f : (float)((sb * kap) & 2047) * (1.0f / 2048.0f);
                const float ca = cosr(aa), sna = sinr(aa), cb = cosr(ab), snb = sinr(ab);
                const float al = ca - snb, be = sna + cb, ga = ca + snb, de = cb - sna;
                const int oo = mirr ? (o ^ 2) : o;
                const float e0 = oo == 0 ? al : oo == 1 ? be : oo == 2 ? ga : de;
                const float e1 = oo == 0 ? -be : oo == 1 ? al : oo == 2 ? de : -ga;
                M3[ks][i] = pk(e0, e1);
            }
    }
    DftC C; dft576_consts(C, l, false, s2, 1.0f / (2048.0f * 576.0f));
#pragma unroll
    for (int m = 0; m < 10; ++m) ldsw<u32x4>(lds + B_SLAB + dst[m], sl[m]);
    __syncthreads();
#pragma unroll
    for (int it = 0; it < 5; ++it) {
        const int tau = 4 * it + w;
        if (tau < 19) {
            int ka, kb;
            if (tau < 18) { const int i = tau / 6, m = tau - 6 * i; kb = 2 * m + (c >> 3); ka = 8 * i + (c & 7); } else { ka = 0; kb = 12; }
            const int ka2 = ka ? 24 - ka : 0, kb2 = ka ? 23 - kb : (kb ? 24 - kb : 0);
            const int pd = pos_norm(ka, kb), pm = pos_norm(ka2, kb2);
            const bool lowk = ka <= 12;
            const int kas = lowk ? ka : ka2, kbs = lowk ? kb : kb2, p0 = pos_spec(0, kas, kbs), p16 = pos_spec(1, kas, kbs);
            const unsigned cj = (g == 0 && !lowk) ? 0x80000000u : 0u;
            LAS char* rb = lds + B_SLAB + 4 * g * (SLABDW * 4);
            u32x4 b0, b1;
            b0[0] = ldsr<unsigned>(rb + (g == 0 ? p0 : pd) * 4) ^ cj;
            b1[0] = ldsr<unsigned>(rb + (g == 0 ? p16 : pm) * 4) ^ cj;
#pragma unroll
            for (int i = 1; i < 4; ++i) { b0[i] = ldsr<unsigned>(rb + i * (SLABDW * 4) + pd * 4); b1[i] = ldsr<unsigned>(rb + i * (SLABDW * 4) + pm * 4); }
            f32x4 d = mfma16(M3[0], b0, (f32x4){});
            d = mfma16(M3[1], b1, d);
            LAS char* zi = lds + B_ZI + g * IMG * 4;
            if (ka != 0 || (kb != 0 && kb != 12)) ldsw<unsigned>(zi + (ka * 28 + kb) * 4, pk(d[2], d[3]));
            ldsw<unsigned>(zi + (ka2 * 28 + kb2) * 4, pk(d[0], d[1]));
        }
    }
    __syncthreads();
    f32x16 oR, oI;
    dft576(lds + B_ZI + w * IMG * 4, l, C, oR, oI);
    const int a = l & 31, h = l >> 5;
    if (a < 24) {
        GAS char* o = (GAS char*)P.out + (size_t)(64 * (8 * j + 2 * w) + s2) * 2304 + 1152 + (24 * a + 4 * h) * 2;
#pragma unroll
        for (int i = 0; i < 3; ++i) {
            *(GAS u32x2*)(o + 16 * i) = (u32x2){pk(oR[4 * i], oR[4 * i + 1]), pk(oR[4 * i + 2], oR[4 * i + 3])};
            *(GAS u32x2*)(o + 16 * i + 64 * 2304) = (u32x2){pk(oI[4 * i], oI[4 * i + 1]), pk(oI[4 * i + 2], oI[4 * i + 3])};
        }
    }
}

DI void load_utile(const Params& P, int a, u32x4 (&ur)[3]) {
    const int tid = threadIdx.x, l = tid & 63, w = rfl(tid >> 6), c = l & 15, g = l >> 4, cg = a & 7, lb = a >> 3;
    const GAS char* src = (const GAS char*)P.u + (size_t)(64 * lb + 16 * w + c) * 1152 + cg * 144;
    ur[0] = *(const GAS u32x4*)(src + 16 * g); ur[1] = *(const GAS u32x4*)(src + 64 + 16 * g); ur[2] = *(const GAS u32x4*)(src + 128);
}
template <int AUX> DI void phase_t1(const Params& P, int a, LAS char* lds, const u32x4 (&ur)[3]) {
    const int tid = threadIdx.x, l = tid & 63, w = rfl(tid >> 6), c = l & 15, g = l >> 4, cg = a & 7, lb = a >> 3;
    LAS char* wp = lds + (16 * w + c) * 176;
    ldsw<u32x4>(wp + 16 * g, ur[0]); ldsw<u32x4>(wp + 64 + 16 * g, ur[1]); if (g == 0) ldsw<u32x4>(wp + 128, ur[2]);
    __syncthreads();
    const rsrc_t rs = ws_rsrc(P);
    const int q = c >> 2, pp = c & 3;
#pragma unroll
    for (int it = 0; it < 3; ++it) {
        const int un = 4 * it + w;
        if (un < 10) {
            const int nu = un >> 1, eta = un & 1;
            LAS char* rp = lds + (32 * eta + 8 * g + q) * 176 + 32 * nu + 8 * pp;
            const u32x2 lo = trread(rp), hi = trread(rp + 4 * 176);
            if (16 * nu + c < 72)
                __builtin_amdgcn_raw_buffer_store_b128((u32x4){lo[0], lo[1], hi[0], hi[1]}, rs, UTOFF + (unsigned)(72 * cg + 16 * nu + c) * 4096 + (unsigned)(64 * lb + 32 * eta + 8 * g) * 2, 0, AUX);
        }
    }
}
struct cf { float r, i; };
DI cf cmul(cf a, cf b) { return {a.r * b.r - a.i * b.i, a.r * b.i + a.i * b.r}; }
DI cf zpow(float al2, float brev, float p) { const float e = __builtin_amdgcn_exp2f(al2 * p), t = brev * p; return {e * cosr(t), e * sinr(t)}; }
DI void kgen_unit(const Params& P, int ch, int half, LAS char* tab, LAS char* kp, int l) {
    const int idx = ch * 128 + half * 64 + l;
    const float la = bflo(((const GAS u16*)P.logA)[idx]), aim = bflo(((const GAS u16*)P.Aim)[idx]);
    const float cr = bflo(((const GAS u16*)P.Cre)[idx]), ci = bflo(((const GAS u16*)P.Cim)[idx]);
    const float dt = bfround(expf(bflo(((const GAS u16*)P.logdt)[ch])));
    const float al2 = -bfround(expf(la)) * dt * 1.44269504088896f, brev = aim * dt * 0.15915494309189535f;
    const cf z1 = zpow(al2, brev, 1.f), z8 = zpow(al2, brev, 8.f), z32 = zpow(al2, brev, 32.f), z256 = zpow(al2, brev, 256.f);
    LAS char* Lp = tab + l * 4; LAS char* Hp = tab + 32 * 272 + l * 4;
    cf L[4]; L[0] = {1.f, 0.f}; L[1] = z8; L[2] = cmul(z8, z8); L[3] = cmul(L[2], z8);
#pragma unroll
    for (int jj = 0; jj < 8; ++jj)
#pragma unroll
        for (int k = 0; k < 4; ++k) { ldsw<unsigned>(Lp + (4 * jj + k) * 272, pk(L[k].r, L[k].i)); if (jj < 7) L[k] = cmul(L[k], z32); }
    cf H[8]; H[0] = {2.f * cr * dt, 2.f * ci * dt};
#pragma unroll
    for (int k = 1; k < 8; ++k) H[k] = cmul(H[k - 1], z1);
#pragma unroll
    for (int k = 0; k < 8; ++k) ldsw<unsigned>(Hp + k * 272, pk(H[k].r, -H[k].i));
#pragma unroll
    for (int k = 0; k < 4; ++k) { H[2 * k] = cmul(H[2 * k], z256); ldsw<unsigned>(Hp + (8 + k) * 272, pk(H[2 * k].r, -H[2 * k].i)); }
#pragma unroll
    for (int k = 0; k < 4; ++k) { const int s = 4 * (k & 1); H[s] = cmul(H[s], z256); ldsw<unsigned>(Hp + (12 + k) * 272, pk(H[s].r, -H[s].i)); }
#pragma unroll
    for (int k = 0; k < 5; ++k) { H[0] = cmul(H[0], z256); ldsw<unsigned>(Hp + (16 + k) * 272, pk(H[0].r, -H[0].i)); }
    const int jc = l & 31, h = l >> 5;
    LAS char* ap = tab + (32 + (jc < 21 ? jc : 20)) * 272 + 16 * h; LAS char* bp = tab + jc * 272 + 16 * h;
    f32x16 d = {};
#pragma unroll
    for (int s = 0; s < 8; ++s) d = mfma32(ldsr<u32x4>(ap + 32 * s), ldsr<u32x4>(bp + 32 * s), d);
    ldsw<f32x4>(kp + (8 * jc + 4 * h) * 4, (f32x4){d[0], d[1], d[2], d[3]});
    ldsw<f32x2>(kp + (h ? 384 + 2 * jc : 256 + 4 * jc) * 4, (f32x2){d[4], d[5]});
    ldsw<f32x2>(kp + (h ? 448 + 2 * jc : 258 + 4 * jc) * 4, (f32x2){d[6], d[7]});
#pragma unroll
    for (int i = 0; i < 4; ++i) ldsw<float>(kp + (512 + 128 * h + 32 * i + jc) * 4, d[8 + i]);
}
DI u32x4 kexpand(LAS char* k0, int w, int tid) {
    LAS char* k1 = k0 + 3072;
    if (w == 0) {
        const int base = (tid < 32 ? 8 * tid : 128 + 4 * tid) * 4;
        const f32x4 a = ldsr<f32x4>(k0 + base) + ldsr<f32x4>(k1 + base), b = ldsr<f32x4>(k0 + base + 16) + ldsr<f32x4>(k1 + base + 16);
        if (tid < 32) return (u32x4){pk(b[3], b[2]), pk(b[1], b[0]), pk(a[3], a[2]), pk(a[1], a[0])};
        return (u32x4){pk(b[0], a[3]), pk(a[2], a[2]), pk(a[2], a[1]), pk(a[0], a[0])};
    } else if (w == 1) {
        const int base = (256 + 2 * tid) * 4;
        const f32x2 a = ldsr<f32x2>(k0 + base) + ldsr<f32x2>(k1 + base); const float b = ldsr<float>(k0 + base + 8) + ldsr<float>(k1 + base + 8);
        return (u32x4){pk(b, b), pk(a[1], a[1]), pk(a[1], a[0]), pk(a[0], a[0])};
    }
    const int base = (384 + tid) * 4;
    const float v0 = ldsr<float>(k0 + base) + ldsr<float>(k1 + base), v1 = ldsr<float>(k0 + base + 4) + ldsr<float>(k1 + base + 4);
    const unsigned lo = pk(v0, v0);
    return (u32x4){pk(v1, v1), pk(v1, (tid & 1) ? v1 : v0), lo, lo};
}
template <int NRES> DI void conv_pass(LAS char* chb, int res0, int l, f32x4 (&acc)[8]) {
    const int r = l & 15, g = l >> 4, cp = (r + 1) & 3;
    LAS char* fp = chb + cp * UCOPY + 2 * (9 + r - 8 * g - cp + 16) + 64 * res0;
    LAS char* fp2 = fp + 8; asm volatile("" : "+v"(fp2));
    LAS char* gp = chb + UCH + 2 * (2056 - 16 * r - 8 * g - 1792) + 64 * res0;
#pragma unroll
    for (int t = 0; t < 8; ++t) acc[t] = (f32x4){};
#pragma unroll
    for (int rho = 0; rho < NRES; ++rho) {
        u32x4 F[8], G[8];
#pragma unroll
        for (int i = 0; i < 8; ++i) {
            const u32x2 a = ldsr<u32x2>(fp + 64 * rho + 512 * i), b = ldsr<u32x2>(fp2 + 64 * rho + 512 * i);
            F[i] = (u32x4){a[0], a[1], b[0], b[1]};
            G[i] = ldsr<u32x4>(gp + 64 * rho + 512 * (7 - i));
        }
#pragma unroll
        for (int t = 0; t < 8; ++t)
#pragma unroll
            for (int i = 0; i <= t; ++i) acc[t] = mfma16(F[i], G[t - i], acc[t]);
    }
}
template <int AUX> DI void store_y(rsrc_t rs, int ch, int t, int l, f32x4 v) {
    const int c = l & 15, g = l >> 4;
    __builtin_amdgcn_raw_buffer_store_b64((u32x2){pk(v[0], v[1]), pk(v[2], v[3])}, rs, YTOFF + (unsigned)ch * 4096 + (unsigned)(256 * t + 16 * c + 4 * g) * 2, 0, AUX);
}
template <int AUX> DI void phase_conv(const Params& P, int a, LAS char* lds) {
    const int tid = threadIdx.x, l = tid & 63, w = rfl(tid >> 6), cg = a & 7, lb = a >> 3;
    const bool three = lb < 8;
    const int ch0 = 72 * cg + (three ? 3 * lb : 8 + 2 * lb), nch = three ? 3 : 2;
    const rsrc_t rs = ws_rsrc(P);
    kgen_unit(P, ch0 + (w >> 1), w & 1, lds + A_TAB + w * TABB, lds + A_KP + w * 3072, l);
    if (three && w < 2) kgen_unit(P, ch0 + 2, w, lds + A_TAB + w * TABB, lds + A_KP + (4 + w) * 3072, l);
    u32x4 ua[3]; u32x2 ub[3];
#pragma unroll
    for (int k = 0; k < 3; ++k) if (k < nch) {
        ua[k] = __builtin_amdgcn_raw_buffer_load_b128(rs, UTOFF + (unsigned)(ch0 + k) * 4096 + tid * 16, 0, AUX);
        ub[k] = __builtin_amdgcn_raw_buffer_load_b64(rs, UTOFF + (unsigned)(ch0 + k) * 4096 + tid * 16 + 16, 0, AUX);
    }
    __syncthreads();
    u32x4 kx[3];
#pragma unroll
    for (int k = 0; k < 3; ++k) if (k < nch) kx[k] = kexpand(lds + A_KP + 2 * k * 3072, w, tid);
    __syncthreads();
#pragma unroll
    for (int k = 0; k < 3; ++k) if (k < nch) {
        LAS char* cb = lds + k * CHB;
        ldsw<u32x4>(cb + UCH + (2040 - 8 * tid) * 2, kx[k]);
        if (tid < 30) ldsw<u32x4>(cb + UCH + 4096 + 16 * tid, (u32x4){0, 0, 0, 0});
        const unsigned d0 = ua[k][0], d1 = ua[k][1], d2 = ua[k][2], d3 = ua[k][3], d4 = ub[k][0], d5 = ub[k][1];
        const unsigned a01 = __builtin_amdgcn_alignbit(d1, d0, 16), a12 = __builtin_amdgcn_alignbit(d2, d1, 16), a23 = __builtin_amdgcn_alignbit(d3, d2, 16),
                       a34 = __builtin_amdgcn_alignbit(d4, d3, 16), a45 = __builtin_amdgcn_alignbit(d5, d4, 16);
        LAS char* up = cb + 32 + 16 * tid;
        ldsw<u32x4>(up, ua[k]); ldsw<u32x4>(up + UCOPY, (u32x4){a01, a12, a23, a34});
        ldsw<u32x4>(up + 2 * UCOPY, (u32x4){d1, d2, d3, d4}); ldsw<u32x4>(up + 3 * UCOPY, (u32x4){a12, a23, a34, a45});
        if (w == 0) {
            const unsigned f0 = rfl(d0), f01 = rfl(a01);
            if (l < 8) {
                const int cc = l >> 1; const bool sec = l & 1;
                const unsigned e3 = !sec ? 0u : cc == 1 ? (f0 << 16) : cc == 2 ? f0 : cc == 3 ? f01 : 0u;
                const unsigned e2 = (sec && cc == 3) ? (f0 << 16) : 0u;
                ldsw<u32x4>(cb + cc * UCOPY + 16 * (l & 1), (u32x4){0, 0, e2, e3});
            }
        }
    }
    __syncthreads();
    f32x4 acc[8];
    conv_pass<4>(lds + (w >> 1) * CHB, 4 * (w & 1), l, acc);
    {
        LAS char* dp = lds + A_DUMP + l * 16;
        if (w & 1) {
#pragma unroll
            for (int t = 0; t < 4; ++t) ldsw<f32x4>(dp + (w * 4 + t) * 1024, acc[t]);
        } else {
#pragma unroll
            for (int t = 0; t < 4; ++t) ldsw<f32x4>(dp + (w * 4 + t) * 1024, acc[4 + t]);
        }
        __syncthreads();
        if (w & 1) {
#pragma unroll
            for (int t = 0; t < 4; ++t) store_y<AUX>(rs, ch0 + (w >> 1), 4 + t, l, acc[4 + t] + ldsr<f32x4>(dp + ((w ^ 1) * 4 + t) * 1024));
        } else {
#pragma unroll
            for (int t = 0; t < 4; ++t) store_y<AUX>(rs, ch0 + (w >> 1), t, l, acc[t] + ldsr<f32x4>(dp + ((w ^ 1) * 4 + t) * 1024));
        }
    }
    if (three) {
        conv_pass<2>(lds + 2 * CHB, 2 * w, l, acc);
        LAS char* dp = lds + l * 16;
#pragma unroll
        for (int t = 0; t < 8; ++t) ldsw<f32x4>(dp + (w * 8 + t) * 1024, acc[t]);
        __syncthreads();
#pragma unroll
        for (int k = 0; k < 2; ++k) {
            const int t = 2 * w + k;
            const f32x4 v = (ldsr<f32x4>(dp + t * 1024) + ldsr<f32x4>(dp + (8 + t) * 1024)) + (ldsr<f32x4>(dp + (16 + t) * 1024) + ldsr<f32x4>(dp + (24 + t) * 1024));
            store_y<AUX>(rs, ch0 + 2, t, l, v);
        }
    }
}
DI void skip_term(const Params& P, int a, u32x4 (&ur)[3]) {
    const int l = threadIdx.x & 63, g = l >> 4, cg = a & 7;
    const GAS char* dsrc = (const GAS char*)P.Dskip + cg * 144;
#pragma unroll
    for (int m = 0; m < 3; ++m) {
        const u32x4 dv = *(const GAS u32x4*)(dsrc + (m < 2 ? 64 * m + 16 * g : 128));
#pragma unroll
        for (int i = 0; i < 4; ++i) ur[m][i] = pk(bflo(ur[m][i]) * bflo(dv[i]), bfhi(ur[m][i]) * bfhi(dv[i]));
    }
}
template <int AUX> DI void phase_t2(const Params& P, int a, LAS char* lds, const u32x4 (&ud)[3]) {
    const int tid = threadIdx.x, l = tid & 63, w = rfl(tid >> 6), c = l & 15, g = l >> 4, cg = a & 7, lb = a >> 3;
    const rsrc_t rs = ws_rsrc(P);
    u32x4 yv[3];
#pragma unroll
    for (int m = 0; m < 3; ++m) {
        const int n = m < 2 ? tid + 256 * m : 512 + (tid & 63);
        yv[m] = __builtin_amdgcn_raw_buffer_load_b128(rs, YTOFF + (unsigned)(72 * cg + (n >> 3)) * 4096 + (unsigned)(128 * lb + 16 * (n & 7)), 0, AUX);
    }
#pragma unroll
    for (int m = 0; m < 3; ++m) {
        const int n = m < 2 ? tid + 256 * m : 512 + (tid & 63);
        if (m < 2 || w == 0) ldsw<u32x4>(lds + (n >> 3) * 144 + 16 * (n & 7), yv[m]);
    }
    __syncthreads();
    const int q = c >> 2, pp = c & 3;
    GAS char* o = (GAS char*)P.out + (size_t)(64 * lb + 16 * w + c) * 2304 + cg * 144;
#pragma unroll
    for (int m = 0; m < 3; ++m) {
        const int kc = m < 2 ? 4 * m + g : 8;
        LAS char* rp = lds + (8 * kc + q) * 144 + 32 * w + 8 * pp;
        const u32x2 lo = trread(rp), hi = trread(rp + 4 * 144);
        const u32x4 y = {lo[0], lo[1], hi[0], hi[1]};
        u32x4 r;
#pragma unroll
        for (int i = 0; i < 4; ++i) r[i] = pk(bflo(y[i]) + bflo(ud[m][i]), bfhi(y[i]) + bfhi(ud[m][i]));
        if (m < 2 || g == 0) *(GAS u32x4*)(o + 16 * kc) = r;
    }
}

__global__ void __launch_bounds__(256, 2) k1(Params P) {
    __shared__ __attribute__((aligned(16))) char smem[LDS_BYTES];
    LAS char* lds = (LAS char*)smem; const int b = blockIdx.x;
    if (b < 256) phase_p1<0>(P, b, lds);
    else { u32x4 ur[3]; load_utile(P, b - 256, ur); phase_t1<0>(P, b - 256, lds, ur); }
}
__global__ void __launch_bounds__(256, 2) k2(Params P) {
    __shared__ __attribute__((aligned(16))) char smem[LDS_BYTES];
    LAS char* lds = (LAS char*)smem; const int b = blockIdx.x;
    if (b < 256) phase_conv<0>(P, b, lds);
    else phase_p2<0>(P, b - 256);
}
__global__ void __launch_bounds__(256, 2) k3(Params P) {
    __shared__ __attribute__((aligned(16))) char smem[LDS_BYTES];
    LAS char* lds = (LAS char*)smem; const int b = blockIdx.x;
    if (b < 256) phase_p3<0>(P, b, lds);
    else { u32x4 ur[3]; load_utile(P, b - 256, ur); skip_term(P, b - 256, ur); phase_t2<0>(P, b - 256, lds, ur); }
}

extern "C" void kernel_launch(void* const* d_in, const int* in_sizes, int n_in, void* d_out, int out_size, void* d_ws, size_t ws_size, hipStream_t stream) {
    Params P;
    P.u = (const u16*)d_in[0]; P.logA = (const u16*)d_in[1]; P.Aim = (const u16*)d_in[2]; P.Cre = (const u16*)d_in[3]; P.Cim = (const u16*)d_in[4];
    P.logdt = (const u16*)d_in[5]; P.Dskip = (const u16*)d_in[6]; P.x = (const u16*)d_in[7]; P.wr = (const u16*)d_in[8]; P.wi = (const u16*)d_in[9];
    P.out = (u16*)d_out; P.ws = (char*)d_ws;
    k1<<<512, 256, 0, stream>>>(P);
    k2<<<256 + 145, 256, 0, stream>>>(P);
    k3<<<512, 256, 0, stream>>>(P);
}
```

```cpp
#include <hip/hip_runtime.h>
#include <hip/hip_bf16.h>
#include <stdint.h>

#define HINT_E1 0
#define HINT_E2 0
#define HINT_UT 0
#define HINT_YT 0
#define NSLOT 1

#define GAS __attribute__((address_space(1)))
#define LAS __attribute__((address_space(3)))
#define DI __device__ __forceinline__
typedef __bf16 bf16x8 __attribute__((ext_vector_type(8)));
typedef __bf16 bf16x2 __attribute__((ext_vector_type(2)));
typedef float f32x2 __attribute__((ext_vector_type(2)));
typedef float f32x4 __attribute__((ext_vector_type(4)));
typedef float f32x16 __attribute__((ext_vector_type(16)));
typedef unsigned u32x2 __attribute__((ext_vector_type(2)));
typedef unsigned u32x4 __attribute__((ext_vector_type(4)));
typedef short s16x4 __attribute__((ext_vector_type(4)));
typedef unsigned short u16;
typedef unsigned long long u64;
typedef __amdgpu_buffer_rsrc_t rsrc_t;
#define RLX_AGENT __ATOMIC_RELAXED, __HIP_MEMORY_SCOPE_AGENT
#define PIN(x) asm volatile("" : "+v"(x))
#define BAR() asm volatile("s_waitcnt lgkmcnt(0)\n\ts_barrier" ::: "memory")
#define DRAIN() asm volatile("s_waitcnt vmcnt(0)" ::: "memory")
constexpr int SC1 = 16;

DI unsigned pk(float a, float b) { f32x2 v = {a, b}; return __builtin_bit_cast(unsigned, __builtin_convertvector(v, bf16x2)); }
DI unsigned pk2(f32x2 v) { return __builtin_bit_cast(unsigned, __builtin_convertvector(v, bf16x2)); }
DI float bflo(unsigned v) { return __builtin_bit_cast(float, v << 16); }
DI float bfhi(unsigned v) { return __builtin_bit_cast(float, v & 0xffff0000u); }
DI float bfround(float x) { return bflo(pk(x, x)); }
DI f32x4 mfma16(u32x4 a, u32x4 b, f32x4 c) { return __builtin_amdgcn_mfma_f32_16x16x32_bf16(__builtin_bit_cast(bf16x8, a), __builtin_bit_cast(bf16x8, b), c, 0, 0, 0); }
DI f32x16 mfma32(u32x4 a, u32x4 b, f32x16 c) { return __builtin_amdgcn_mfma_f32_32x32x16_bf16(__builtin_bit_cast(bf16x8, a), __builtin_bit_cast(bf16x8, b), c, 0, 0, 0); }
DI float cosr(float x) { return __builtin_amdgcn_cosf(x); }
DI float sinr(float x) { return __builtin_amdgcn_sinf(x); }
DI u32x2 trread(LAS char* p) { return __builtin_bit_cast(u32x2, __builtin_amdgcn_ds_read_tr16_b64_v4i16((LAS s16x4*)p)); }
template <class T> DI T ldsr(LAS char* p) { return *(LAS T*)p; }
template <class T> DI void ldsw(LAS char* p, T v) { *(LAS T*)p = v; }
DI int rfl(int x) { return __builtin_amdgcn_readfirstlane(x); }
DI f32x2 cmul(f32x2 a, f32x2 b) { const f32x2 t = a * b.xx; return __builtin_elementwise_fma(a.yx, (f32x2){-b.y, b.y}, t); }
DI unsigned max4(u32x4 v) { return max(max(v[0], v[1]), max(v[2], v[3])); }

struct Params {
    const u16 *u, *logA, *Aim, *Cre, *Cim, *logdt, *Dskip, *x, *wr, *wi;
    u16* out; char* ws;
};

constexpr unsigned ROWB = 2560;
constexpr unsigned E1OFF = 0, E2OFF = 1024 * ROWB, UTOFF = 2048 * ROWB, YTOFF = UTOFF + 576 * 4096 + 4096, FLOFF = YTOFF + 576 * 4096;
constexpr unsigned F_E1 = FLOFF, F_E2 = F_E1 + 1024 * 64, F_UT = F_E2 + 256 * 64, F_YT = F_UT + 256 * 64, SLOT_USED = F_YT + 320 * 64;
constexpr unsigned SLOTB = 10u << 20, EPOCH_OFF = 200u << 20;
static_assert(SLOT_USED <= SLOTB, "slot");
constexpr int B_XM = 0, B_XT = 32768, B_SPEC = 36864, B_SLAB = 0, SLABB = 2576, B_P2T = 0, B_ZI = 55744, IMGB = 2704, B_TAB = 66560;
constexpr int T_PQ = B_TAB, T_TW = B_TAB + 6144, T_M1 = B_TAB + 12288, T_M3 = B_TAB + 13312;
constexpr int UCOPY = 4160, UCH = 4 * UCOPY, KREVB = 4576, CHB = UCH + KREVB, TABB = 53 * 272;
constexpr int LDS_BYTES = 81920;
constexpr unsigned SPIN_CAP = 1u << 18;
constexpr u32x4 INVALID = {~0u, ~0u, ~0u, ~0u};

#define STAMP(k)
#define STAMP_ARG
#define STAMP_PASS

struct Ctx { rsrc_t rs; GAS char* slot; u64 tag; };

DI void publish(const Ctx& X, unsigned off) { __hip_atomic_store((GAS u64*)(X.slot + off), X.tag, RLX_AGENT); }
DI void wait_flags(const Ctx& X, unsigned off) {
#pragma unroll 1
    for (unsigned sp = 0; sp < SPIN_CAP; ++sp) {
        const u64 f = __hip_atomic_load((GAS u64*)(X.slot + off), RLX_AGENT);
        if (__all(f == X.tag)) break;
        __builtin_amdgcn_s_sleep(1);
    }
    __builtin_amdgcn_fence(__ATOMIC_ACQUIRE, "wavefront");
}
DI void wait_flags3(const Ctx& X, unsigned o0, unsigned o1, unsigned o2) {
#pragma unroll 1
    for (unsigned sp = 0; sp < SPIN_CAP; ++sp) {
        const u64 f0 = __hip_atomic_load((GAS u64*)(X.slot + o0), RLX_AGENT), f1 = __hip_atomic_load((GAS u64*)(X.slot + o1), RLX_AGENT), f2 = __hip_atomic_load((GAS u64*)(X.slot + o2), RLX_AGENT);
        if (__all(f0 == X.tag && f1 == X.tag && f2 == X.tag)) break;
        __builtin_amdgcn_s_sleep(1);
    }
    __builtin_amdgcn_fence(__ATOMIC_ACQUIRE, "wavefront");
}

struct DftC { u32x4 P1[3], Q1[3], P2[3], Q2[3]; f32x4 tc[3], ts[3]; };
DI void dft_load(DftC& C, LAS char* pq1, LAS char* pq2, LAS char* tw, int l) {
#pragma unroll
    for (int s = 0; s < 3; ++s) {
        C.P1[s] = ldsr<u32x4>(pq1 + l * 16 + s * 1024); C.Q1[s] = ldsr<u32x4>(pq1 + l * 16 + (3 + s) * 1024);
        C.P2[s] = ldsr<u32x4>(pq2 + l * 16 + s * 1024); C.Q2[s] = ldsr<u32x4>(pq2 + l * 16 + (3 + s) * 1024);
        C.tc[s] = ldsr<f32x4>(tw + l * 16 + s * 1024); C.ts[s] = ldsr<f32x4>(tw + l * 16 + (3 + s) * 1024);
    }
#pragma unroll
    for (int s = 0; s < 3; ++s) { PIN(C.P1[s]); PIN(C.Q1[s]); PIN(C.P2[s]); PIN(C.Q2[s]); PIN(C.tc[s]); PIN(C.ts[s]); }
}
DI void dft576(LAS char* p, const DftC& C, f32x16& oR, f32x16& oI) {
    f32x16 aR = {}, aI = {};
    u32x4 z[3];
#pragma unroll
    for (int s = 0; s < 3; ++s) z[s] = ldsr<u32x4>(p + 32 * s);
#pragma unroll
    for (int s = 0; s < 3; ++s) { aR = mfma32(z[s], C.P1[s], aR); aI = mfma32(z[s], C.Q1[s], aI); }
    oR = (f32x16){}; oI = (f32x16){};
#pragma unroll
    for (int s = 0; s < 3; ++s) {
        u32x4 y;
#pragma unroll
        for (int k = 0; k < 2; ++k) {
            const f32x2 r = {aR[4 * s + 2 * k], aR[4 * s + 2 * k + 1]}, i = {aI[4 * s + 2 * k], aI[4 * s + 2 * k + 1]};
            const f32x2 c = {C.tc[s][2 * k], C.tc[s][2 * k + 1]}, sn = {C.ts[s][2 * k], C.ts[s][2 * k + 1]};
            const f32x2 yr = __builtin_elementwise_fma(i, sn, r * c), yi = __builtin_elementwise_fma(-r, sn, i * c);
            y[2 * k] = pk(yr[0], yi[0]); y[2 * k + 1] = pk(yr[1], yi[1]);
        }
        oR = mfma32(y, C.P2[s], oR); oI = mfma32(y, C.Q2[s], oI);
    }
}

DI void p1_issue(const Params& P, int s2, int w, int l, LAS char* lds) {
    const GAS char* xb = (const GAS char*)P.x + (size_t)(512 * w + s2) * 1152;
#pragma unroll
    for (int i = 0; i < 8; ++i)
        __builtin_amdgcn_global_load_lds((const GAS void*)(xb + i * (64 * 1152) + ((l ^ (2 * i)) * 16)), (LAS void*)(lds + B_XM + (8 * w + i) * 1024), 16, 0, 0);
    const int rr = l >> 3;
    __builtin_amdgcn_global_load_lds((const GAS void*)(xb + rr * (64 * 1152) + 1024 + (((l & 7) ^ (rr & 6)) * 16)), (LAS void*)(lds + B_XT + w * 1024), 16, 0, 0);
}
DI void p1_tables(int s2, int j, int w, int l, LAS char* lds) {
    const int n = l & 31, h = l >> 5, c = l & 15, g = l >> 4;
    LAS char* tp = lds + l * 16 + w * 4;
#pragma unroll
    for (int s = 0; s < 3; ++s) {
        const float f = (float)((8 * s + 4 * h + w) * n), a1 = f * (1.0f / 24.0f), a2 = f * (1.0f / 576.0f);
        const float c1 = cosr(a1), s1 = sinr(a1);
        ldsw<unsigned>(tp + T_PQ + s * 1024, pk(c1, s1)); ldsw<unsigned>(tp + T_PQ + (3 + s) * 1024, pk(-s1, c1));
        ldsw<float>(tp + T_TW + s * 1024, cosr(a2)); ldsw<float>(tp + T_TW + (3 + s) * 1024, sinr(a2));
    }
    {
        const int i = c & 3, kap = 4 * j + (c >> 2), s1 = (w < 2 ? 0 : 12) + 4 * g + 2 * w;
        const float q = i == 1 ? 0.25f : 0.f;
        float a0 = (float)(((64 * s1 + s2) * kap) & 2047) * (1.0f / 2048.0f) + q, a1 = (float)(((64 * s1 + 64 + s2) * kap) & 2047) * (1.0f / 2048.0f) + q;
        if (kap == 0 && i == 1) { a0 = 0.f; a1 = 0.5f; }
        ldsw<unsigned>(tp + T_M1, i < 2 ? pk(cosr(a0), cosr(a1)) : 0u);
    }
    if (j == 0) {
        const bool hi = n < 16 ? n >= 8 : n < 24 ? n >= 20 : (n & 1);
        const int ka = n < 16 ? (n & 7) : n < 24 ? 8 + (n & 3) : 12;
        const float extra = hi ? (float)s2 * (1.0f / 128.0f) : 0.f;
#pragma unroll
        for (int s = 0; s < 3; ++s) {
            const float f = (float)((8 * s + 4 * h + w) * ka), a1 = f * (1.0f / 24.0f), a2 = f * (1.0f / 576.0f) + extra;
            const unsigned q = pk(-sinr(a1), cosr(a1));
            ldsw<unsigned>(tp + B_SPEC + s * 1024, hi ? (q & 0xffff0000u) : (q >> 16));
            ldsw<unsigned>(tp + B_SPEC + (3 + s) * 1024, hi ? (q << 16) : (q & 0xffffu));
            ldsw<float>(tp + B_SPEC + (6 + s) * 1024, cosr(a2)); ldsw<float>(tp + B_SPEC + (9 + s) * 1024, sinr(a2));
        }
    }
}
DI void phase_p1(const Ctx& X, int s2, int j, int w, int l, LAS char* lds STAMP_ARG) {
    const int c = l & 15, g = l >> 4, q = c >> 2, pp = c & 3, r = 4 * g + q;
    LAS char* x0 = lds + B_XM + r * 1024 + (2 * (w ^ q) + (pp >> 1)) * 16 + 8 * (pp & 1);
    LAS char* xe = x0 + 128 * (g & 1); LAS char* xo = x0 - 128 * (g & 1);
    LAS char* xt = lds + B_XT + r * 128 + ((2 * w + (pp >> 1)) ^ (r & 6)) * 16 + 8 * (pp & 1);
    LAS char* za[3];
#pragma unroll
    for (int m = 0; m < 3; ++m) { const int d = 64 * m + 16 * w + c, a = d / 24, bb = d - 24 * a; za[m] = lds + B_ZI + g * IMGB + (bb * 28 + a) * 4; PIN(za[m]); }
    const bool special = (j == 0 && w == 0);
    const int kb = l & 31, h = l >> 5;
    LAS char* zr = lds + B_ZI + w * IMGB + ((kb < 24 ? kb : 23) * 28 + 4 * h) * 4;
    const unsigned rowoff = E1OFF + (unsigned)((4 * j + w) * 64 + s2) * ROWB;
    unsigned so = rowoff + (unsigned)(kb * 8 + h * 4) * 4, so2 = special ? rowoff + (unsigned)(384 + 96 * h + 4 * kb) * 4 : so + 1536;
    PIN(xe); PIN(xo); PIN(xt); PIN(zr); PIN(so); PIN(so2);
    STAMP(1);
    asm volatile("s_waitcnt vmcnt(0) lgkmcnt(0)\n\ts_barrier" ::: "memory");
    STAMP(2);
    {
        const u32x4 M1 = ldsr<u32x4>(lds + T_M1 + l * 16);
        u32x2 lo[9], hi[9];
#pragma unroll
        for (int m = 0; m < 8; ++m) { LAS char* p = ((m & 1) ? xo : xe) + 128 * m; lo[m] = trread(p); hi[m] = trread(p + 16384); }
        lo[8] = trread(xt); hi[8] = trread(xt + 2048);
#pragma unroll
        for (int m = 0; m < 9; ++m) {
            const f32x4 d = mfma16(M1, (u32x4){lo[m][0], lo[m][1], hi[m][0], hi[m][1]}, (f32x4){});
            ldsw<unsigned>(za[m % 3] + 32 * (m / 3), pk(d[0], d[1]));
        }
    }
    DftC C;
    dft_load(C, lds + (special ? B_SPEC : T_PQ), lds + T_PQ, lds + (special ? B_SPEC + 6144 : T_TW), l);
    BAR();
    STAMP(3);
    f32x16 oR, oI;
    dft576(zr, C, oR, oI);
    if (kb < 24) {
#pragma unroll
        for (int i = 0; i < 3; ++i) {
            const u32x4 v = {pk(oR[4 * i], oI[4 * i]), pk(oR[4 * i + 1], oI[4 * i + 1]), pk(oR[4 * i + 2], oI[4 * i + 2]), pk(oR[4 * i + 3], oI[4 * i + 3])};
            __builtin_amdgcn_raw_buffer_store_b128(v, X.rs, i < 2 ? so + 768 * i : so2, 0, SC1);
        }
    }
    if (special && h == 0) {
        __builtin_amdgcn_raw_buffer_store_b32(pk(oR[12], oI[12]), X.rs, rowoff + (unsigned)(576 + kb) * 4, 0, SC1);
        __builtin_amdgcn_raw_buffer_store_b32(pk(oR[13], oI[13]), X.rs, rowoff + (unsigned)(608 + kb) * 4, 0, SC1);
    }
    STAMP(4);
}

DI void radix4(f32x4 (&R)[4], f32x4 (&I)[4]) {
    const f32x4 aR = R[0] + R[2], aI = I[0] + I[2], bR = R[0] - R[2], bI = I[0] - I[2];
    const f32x4 cR = R[1] + R[3], cI = I[1] + I[3], dR = R[1] - R[3], dI = I[1] - I[3];
    R[0] = aR + cR; I[0] = aI + cI; R[2] = aR - cR; I[2] = aI - cI;
    R[1] = bR + dI; I[1] = bI - dR; R[3] = bR - dI; I[3] = bI + dR;
}
DI void p2_geom(int y, int w, int& row, int& pos0) {
    if (y < 135) { row = 1 + y / 9; pos0 = 16 * (4 * (y % 9) + w); } else { row = 0; pos0 = 16 * (4 * (y - 135) + w); }
}
DI void phase_p2(const Params& P, const Ctx& X, int y, int w, int l, LAS char* lds STAMP_ARG) {
    int row, pos0; p2_geom(y, w, row, pos0);
    const int c = l & 15, g = l >> 4, p = pos0 + c;
    int kap = row, ka, kb;
    if (row != 0) { const int i = pos0 / 192, rem = p - 192 * i; kb = rem >> 3; ka = 8 * i + (rem & 7); }
    else if (pos0 < 384) { kap = pos0 >= 192 ? 16 : 0; const int rem = p - (pos0 >= 192 ? 192 : 0); kb = rem >> 3; ka = rem & 7; }
    else if (pos0 < 576) { kap = pos0 >= 480 ? 16 : 0; const int rem = p - (pos0 >= 480 ? 480 : 384); kb = rem >> 2; ka = 8 + (rem & 3); }
    else { kap = pos0 >= 608 ? 16 : 0; kb = (p & 31) < 24 ? (p & 31) : (p & 31) - 24; ka = 12; }
    const int kd = ka + 24 * kb;
    const bool mir = kd > 288, edge = (kd == 0 || kd == 288);
    const int col = mir ? 576 - kd : kd;
    const rsrc_t rwr = __builtin_amdgcn_make_buffer_rsrc((void*)P.wr, 0, 2048 * 578, 0x00020000), rwi = __builtin_amdgcn_make_buffer_rsrc((void*)P.wi, 0, 2048 * 578, 0x00020000);
    const int k0 = kap + 128 * g;
    f32x4 Wr[4], Wi[4];
    {
        u16 a[16], bq[16];
        const int base = (mir ? 2048 - k0 : k0) * 578 + 2 * col, step = mir ? -32 * 578 : 32 * 578;
#pragma unroll
        for (int t = 0; t < 16; ++t) {
            int off = base + step * (16 * (t >> 2) + (t & 3));
            if (t == 0) off = (mir && k0 == 0) ? 2 * col : off;
            a[t] = __builtin_amdgcn_raw_buffer_load_b16(rwr, off, 0, 0); bq[t] = __builtin_amdgcn_raw_buffer_load_b16(rwi, off, 0, 0);
        }
        {
            const int uu = 4 * g + w;
            LAS char* tp = lds + B_P2T + l * 16 + w * 4;
#pragma unroll
            for (int t = 0; t < 4; ++t) {
                const float a1 = (float)(c * (t + 4 * uu)) * (1.0f / 64.0f), a2 = (float)(uu * (t + 4 * c)) * (1.0f / 64.0f);
                const float c1 = cosr(a1), s1 = sinr(a1), c2 = cosr(a2), s2 = sinr(a2);
                ldsw<unsigned>(tp + t * 1024, pk(c1, s1)); ldsw<unsigned>(tp + (4 + t) * 1024, pk(-s1, c1));
                ldsw<unsigned>(tp + (8 + t) * 1024, pk(c2, s2)); ldsw<unsigned>(tp + (12 + t) * 1024, pk(s2, -c2));
                if (row == 0) {
                    const float a3 = a2 + (float)(t + 4 * c) * (1.0f / 128.0f), c3 = cosr(a3), s3 = sinr(a3);
                    ldsw<unsigned>(tp + (16 + t) * 1024, pk(c3, s3)); ldsw<unsigned>(tp + (20 + t) * 1024, pk(s3, -c3));
                }
            }
        }
        const unsigned sg = mir ? 0x80000000u : 0u;
#pragma unroll
        for (int t = 0; t < 16; ++t) { Wr[t >> 2][t & 3] = bflo(a[t]); Wi[t >> 2][t & 3] = __builtin_bit_cast(float, ((unsigned)bq[t] << 16) ^ sg); }
        if (__any(edge)) {
            const int base2 = (2048 - k0) * 578 + 2 * col;
#pragma unroll
            for (int t = 0; t < 16; ++t) {
                int off = base2 - 32 * 578 * (16 * (t >> 2) + (t & 3));
                if (t == 0) off = k0 == 0 ? 2 * col : off;
                a[t] = __builtin_amdgcn_raw_buffer_load_b16(rwr, off, 0, 0); bq[t] = __builtin_amdgcn_raw_buffer_load_b16(rwi, off, 0, 0);
            }
            const float e0 = edge ? 0.5f : 1.0f, e1 = edge ? 0.5f : 0.0f;
#pragma unroll
            for (int t = 0; t < 16; ++t) { Wr[t >> 2][t & 3] = e0 * Wr[t >> 2][t & 3] + e1 * bflo(a[t]); Wi[t >> 2][t & 3] = e0 * Wi[t >> 2][t & 3] - e1 * bflo(bq[t]); }
        }
    }
    BAR();
    u32x4 Pf[4], Qf[4], Pi[4], Qi[4];
    {
        LAS char* tp = lds + B_P2T + l * 16; LAS char* ti = tp + (kap == 16 ? 16 * 1024 : 8 * 1024);
#pragma unroll
        for (int t = 0; t < 4; ++t) { Pf[t] = ldsr<u32x4>(tp + t * 1024); Qf[t] = ldsr<u32x4>(tp + (4 + t) * 1024); Pi[t] = ldsr<u32x4>(ti + t * 1024); Qi[t] = ldsr<u32x4>(ti + (4 + t) * 1024); }
#pragma unroll
        for (int t = 0; t < 4; ++t) { PIN(Pf[t]); PIN(Qf[t]); PIN(Pi[t]); PIN(Qi[t]); PIN(Wr[t]); PIN(Wi[t]); }
    }
    unsigned lo = E1OFF + (unsigned)(row * 64 + 16 * g) * ROWB + (unsigned)p * 4;
    unsigned so = E2OFF + (unsigned)(4 * c * 16 + row) * ROWB + (unsigned)(pos0 + 4 * g) * 4;
    PIN(lo); PIN(so);
    STAMP(5);
    wait_flags(X, F_E1 + (unsigned)(row * 64 + l) * 64);
    STAMP(6);
    u32x4 Xv[4];
#pragma unroll 1
    for (unsigned sp = 0; sp < SPIN_CAP; ++sp) {
#pragma unroll
        for (int t = 0; t < 4; ++t)
#pragma unroll
            for (int i = 0; i < 4; ++i) Xv[t][i] = __builtin_amdgcn_raw_buffer_load_b32(X.rs, lo, (t + 4 * i) * ROWB, SC1);
        if (!HINT_E1) break;
        if (__all(max(max(max4(Xv[0]), max4(Xv[1])), max(max4(Xv[2]), max4(Xv[3]))) != ~0u)) break;
        asm volatile("" ::: "memory");
    }
    STAMP(7);
    f32x4 R[4], I[4];
#pragma unroll
    for (int t = 0; t < 4; ++t) { R[t] = mfma16(Pf[t], Xv[t], (f32x4){}); I[t] = mfma16(Qf[t], Xv[t], (f32x4){}); }
    radix4(R, I);
#pragma unroll
    for (int t = 0; t < 4; ++t) { const f32x4 zr = R[t] * Wr[t] - I[t] * Wi[t], zi = -(R[t] * Wi[t]) - I[t] * Wr[t]; R[t] = zr; I[t] = zi; }
    radix4(R, I);
#pragma unroll
    for (int t = 0; t < 4; ++t) {
        const u32x4 hq = {pk(R[t][0], I[t][0]), pk(R[t][1], I[t][1]), pk(R[t][2], I[t][2]), pk(R[t][3], I[t][3])};
        const f32x4 oR = mfma16(hq, Pi[t], (f32x4){}), oI = mfma16(hq, Qi[t], (f32x4){});
        const u32x4 v = {pk(oR[0], oI[0]), pk(oR[1], oI[1]), pk(oR[2], oI[2]), pk(oR[3], oI[3])};
        __builtin_amdgcn_raw_buffer_store_b128(v, X.rs, so, t * 16 * ROWB, SC1);
    }
    STAMP(8);
    DRAIN(); BAR();
    if (threadIdx.x == 0) publish(X, F_E2 + (unsigned)y * 64);
}

DI int pos_norm(int ka, int kb) { return 192 * (ka >> 3) + 8 * kb + (ka & 7); }
DI void phase_p3(const Params& P, const Ctx& X, int s2, int j, int w, int l, LAS char* lds STAMP_ARG) {
    const int tid = threadIdx.x, c = l & 15, g = l >> 4;
    {
        const int pr = c >> 2, o = c & 3, sa = 64 * (8 * j + 2 * pr) + s2, m = 4 * g + w;
        const float aa = (float)((sa * m) & 2047) * (1.0f / 2048.0f), ab = (float)(((sa + 64) * m) & 2047) * (1.0f / 2048.0f);
        const float sc = 1.0f / (2048.0f * 576.0f);
        const float ca = cosr(aa) * sc, sna = sinr(aa) * sc, cb = cosr(ab) * sc, snb = sinr(ab) * sc;
        const float al = ca - snb, be = sna + cb, ga = ca + snb, de = cb - sna;
        const unsigned v0 = pk(al, -be), v1 = pk(be, al), v2 = pk(ga, de), v3 = pk(de, -ga);
        const unsigned lo = (o & 1) ? v1 : v0, hi = (o & 1) ? v3 : v2;
        const unsigned s16 = o == 0 ? pk(sc, sc) : o == 1 ? pk(-sc, sc) : o == 2 ? pk(sc, -sc) : pk(-sc, -sc);
        ldsw<unsigned>(lds + T_M3 + l * 16 + w * 4, (o & 2) ? hi : lo);
        ldsw<unsigned>(lds + T_M3 + 1024 + l * 16 + w * 4, m == 0 ? s16 : (o & 2) ? lo : hi);
    }
    unsigned go[10]; LAS char* dst[10];
#pragma unroll
    for (int m = 0; m < 10; ++m) {
        int r, ch;
        if (m < 9) { const int n = m * 256 + tid; r = n / 144; ch = n - 144 * r; } else { r = 0; ch = 144 + (tid & 15); }
        go[m] = E2OFF + (unsigned)(s2 * 16 + r) * ROWB + (unsigned)ch * 16; dst[m] = lds + B_SLAB + r * SLABB + ch * 16;
        PIN(go[m]); PIN(dst[m]);
    }
    LAS char *aD[5], *aM[5], *a0[5], *a16[5], *zp[5], *zm[5]; unsigned cj[5];
#pragma unroll
    for (int it = 0; it < 5; ++it) {
        const int tau = 4 * it + w;
        int ka, kb;
        if (tau < 18) { const int i = tau / 6, m = tau - 6 * i; kb = 2 * m + (c >> 3); ka = 8 * i + (c & 7); } else { ka = 0; kb = 12; }
        const int ka2 = ka ? 24 - ka : 0, kb2 = ka ? 23 - kb : (kb ? 24 - kb : 0);
        const bool lowk = ka <= 12;
        const int kas = lowk ? ka : ka2, kbs = lowk ? kb : kb2;
        const int p0 = kas < 8 ? 8 * kbs + kas : kas < 12 ? 376 + 4 * kbs + kas : 576 + kbs, dp = kas < 8 ? 192 : kas < 12 ? 96 : 32;
        LAS char* rb = lds + B_SLAB + 4 * g * SLABB;
        aD[it] = rb + pos_norm(ka, kb) * 4; aM[it] = rb + pos_norm(ka2, kb2) * 4;
        a0[it] = g == 0 ? rb + p0 * 4 : aD[it]; a16[it] = g == 0 ? rb + (p0 + dp) * 4 : aM[it];
        cj[it] = (g == 0 && !lowk) ? 0x80000000u : 0u;
        LAS char* zi = lds + B_ZI + g * IMGB;
        zp[it] = zi + (ka2 * 28 + kb2) * 4;
        zm[it] = (ka == 0 && (kb == 0 || kb == 12)) ? zi + 24 * 4 : zi + (ka * 28 + kb) * 4;
        PIN(aD[it]); PIN(aM[it]); PIN(a0[it]); PIN(a16[it]); PIN(zp[it]); PIN(zm[it]); PIN(cj[it]);
    }
    DftC C;
    dft_load(C, lds + T_PQ, lds + T_PQ, lds + T_TW, l);
    const int a = l & 31, h = l >> 5;
    LAS char* zr = lds + B_ZI + w * IMGB + ((a < 24 ? a : 23) * 28 + 4 * h) * 4;
    unsigned oo = (unsigned)(64 * (8 * j + 2 * w) + s2) * 2304 + 1152 + (24 * a + 4 * h) * 2;
    PIN(zr); PIN(oo);
    STAMP(10);
    if (w == 0) wait_flags3(X, F_E2 + (unsigned)l * 64, F_E2 + (unsigned)(64 + l) * 64, F_E2 + (unsigned)(l < 17 ? 128 + l : 0) * 64);
    BAR();
    const u32x4 M3a = ldsr<u32x4>(lds + T_M3 + l * 16), M3b = ldsr<u32x4>(lds + T_M3 + 1024 + l * 16);
    STAMP(11);
    {
        u32x4 sl[10];
#pragma unroll 1
    for (unsigned sp = 0; sp < SPIN_CAP; ++sp) {
#pragma unroll
            for (int m = 0; m < 10; ++m) sl[m] = __builtin_amdgcn_raw_buffer_load_b128(X.rs, go[m], 0, SC1);
            if (!HINT_E2) break;
            unsigned mx = 0;
#pragma unroll
            for (int m = 0; m < 10; ++m) mx = max(mx, max4(sl[m]));
            if (__all(mx != ~0u)) break;
            asm volatile("" ::: "memory");
        }
#pragma unroll
        for (int m = 0; m < 10; ++m) ldsw<u32x4>(dst[m], sl[m]);
    }
    BAR();
    STAMP(12);
    {
        u32x4 b0[5], b1[5];
#pragma unroll
        for (int it = 0; it < 5; ++it) {
            b0[it][0] = ldsr<unsigned>(a0[it]); b1[it][0] = ldsr<unsigned>(a16[it]);
#pragma unroll
            for (int i = 1; i < 4; ++i) { b0[it][i] = ldsr<unsigned>(aD[it] + i * SLABB); b1[it][i] = ldsr<unsigned>(aM[it] + i * SLABB); }
        }
#pragma unroll
        for (int it = 0; it < 5; ++it) {
            b0[it][0] ^= cj[it]; b1[it][0] ^= cj[it];
            f32x4 d = mfma16(M3a, b0[it], (f32x4){});
            d = mfma16(M3b, b1[it], d);
            ldsw<unsigned>(zm[it], pk(d[2], d[3]));
            ldsw<unsigned>(zp[it], pk(d[0], d[1]));
        }
    }
    BAR();
    STAMP(13);
    f32x16 oR, oI;
    dft576(zr, C, oR, oI);
    const rsrc_t ro = __builtin_amdgcn_make_buffer_rsrc((void*)P.out, 0, 2048 * 2304, 0x00020000);
    if (a < 24) {
#pragma unroll
        for (int i = 0; i < 3; ++i) {
            __builtin_amdgcn_raw_buffer_store_b64((u32x2){pk(oR[4 * i], oR[4 * i + 1]), pk(oR[4 * i + 2], oR[4 * i + 3])}, ro, oo + 16 * i, 0, 0);
            __builtin_amdgcn_raw_buffer_store_b64((u32x2){pk(oI[4 * i], oI[4 * i + 1]), pk(oI[4 * i + 2], oI[4 * i + 3])}, ro, oo + 16 * i, 64 * 2304, 0);
        }
    }
    STAMP(14);
}

DI void phase_t1(const Ctx& X, int cg, int lb, int w, int l, LAS char* lds, const u32x4 (&ur)[3] STAMP_ARG) {
    const int c = l & 15, g = l >> 4, q = c >> 2, pp = c & 3;
    LAS char* wp = lds + (16 * w + c) * 176 + 16 * g;
    LAS char* rp = lds + (32 * (w & 1) + 8 * g + q) * 176 + 32 * (w >> 1) + 8 * pp;
    unsigned so = UTOFF + (unsigned)(72 * cg + 16 * (w >> 1) + c) * 4096 + (unsigned)(64 * lb + 32 * (w & 1) + 8 * g) * 2;
    PIN(wp); PIN(rp); PIN(so);
    ldsw<u32x4>(wp, ur[0]); ldsw<u32x4>(wp + 64, ur[1]); if (g == 0) ldsw<u32x4>(wp + 128, ur[2]);
    BAR();
#pragma unroll
    for (int it = 0; it < 3; ++it) {
        if (it < 2 || w < 2) {
            const u32x2 lo = trread(rp + 64 * it), hi = trread(rp + 64 * it + 4 * 176);
            if (it < 2 || c < 8) __builtin_amdgcn_raw_buffer_store_b128((u32x4){lo[0], lo[1], hi[0], hi[1]}, X.rs, so, it * 32 * 4096, SC1);
        }
    }
    BAR();
}
DI f32x2 zpow(float al2, float brev, float p) { const float e = __builtin_amdgcn_exp2f(al2 * p), t = __builtin_amdgcn_fractf(brev * p); return (f32x2){e * cosr(t), e * sinr(t)}; }
struct Modes { unsigned la, aim, cr, ci, ldt; };
DI Modes load_modes(const Params& P, int ch, int half, int l) {
    const int idx = ch * 128 + half * 64 + l;
    return {((const GAS u16*)P.logA)[idx], ((const GAS u16*)P.Aim)[idx], ((const GAS u16*)P.Cre)[idx], ((const GAS u16*)P.Cim)[idx], ((const GAS u16*)P.logdt)[ch]};
}
DI void kgen_tables(const Modes& M, LAS char* tab, int l) {
    const float dt = bfround(expf(bflo(M.ldt)));
    const float al2 = -bfround(expf(bflo(M.la))) * dt * 1.44269504088896f, brev = bflo(M.aim) * dt * 0.15915494309189535f;
    const f32x2 z8 = zpow(al2, brev, 8.f), z32 = zpow(al2, brev, 32.f), y1 = zpow(al2, -brev, 1.f), y256 = zpow(al2, -brev, 256.f);
    LAS char* Lp = tab + l * 4; LAS char* Hp = tab + 32 * 272 + l * 4;
    f32x2 L[4]; L[0] = (f32x2){1.f, 0.f}; L[1] = z8; L[2] = cmul(z8, z8); L[3] = cmul(L[2], z8);
    f32x2 H[8]; H[0] = (f32x2){bflo(M.cr), -bflo(M.ci)} * (2.f * dt);
#pragma unroll
    for (int jj = 0; jj < 8; ++jj) {
#pragma unroll
        for (int k = 0; k < 4; ++k) { ldsw<unsigned>(Lp + (4 * jj + k) * 272, pk2(L[k])); if (jj < 7) L[k] = cmul(L[k], z32); }
        ldsw<unsigned>(Hp + jj * 272, pk2(H[jj])); if (jj < 7) H[jj + 1] = cmul(H[jj], y1);
    }
#pragma unroll
    for (int k = 0; k < 4; ++k) { H[2 * k] = cmul(H[2 * k], y256); ldsw<unsigned>(Hp + (8 + k) * 272, pk2(H[2 * k])); }
#pragma unroll
    for (int k = 0; k < 4; ++k) { const int s = 4 * (k & 1); H[s] = cmul(H[s], y256); ldsw<unsigned>(Hp + (12 + k) * 272, pk2(H[s])); }
#pragma unroll
    for (int k = 0; k < 5; ++k) { H[0] = cmul(H[0], y256); ldsw<unsigned>(Hp + (16 + k) * 272, pk2(H[0])); }
}
DI void kgen_contract(LAS char* tab, LAS char* kp, int l) {
    const int jc = l & 31, h = l >> 5;
    LAS char* ap = tab + (32 + (jc < 21 ? jc : 20)) * 272 + 16 * h; LAS char* bp = tab + jc * 272 + 16 * h;
    u32x4 A[8], B[8];
#pragma unroll
    for (int s = 0; s < 8; ++s) { A[s] = ldsr<u32x4>(ap + 32 * s); B[s] = ldsr<u32x4>(bp + 32 * s); }
    f32x16 d = {};
#pragma unroll
    for (int s = 0; s < 8; ++s) d = mfma32(A[s], B[s], d);
    ldsw<f32x4>(kp + (8 * jc + 4 * h) * 4, (f32x4){d[0], d[1], d[2], d[3]});
    ldsw<f32x2>(kp + (h ? 384 + 2 * jc : 256 + 4 * jc) * 4, (f32x2){d[4], d[5]});
    ldsw<f32x2>(kp + (h ? 448 + 2 * jc : 258 + 4 * jc) * 4, (f32x2){d[6], d[7]});
#pragma unroll
    for (int i = 0; i < 4; ++i) ldsw<float>(kp + (512 + 128 * h + 32 * i + jc) * 4, d[8 + i]);
}
DI u32x4 kexpand(LAS char* k0, int w, int tid) {
    LAS char* k1 = k0 + 3072;
    if (w == 0) {
        const int base = (tid < 32 ? 8 * tid : 128 + 4 * tid) * 4;
        const f32x4 a = ldsr<f32x4>(k0 + base) + ldsr<f32x4>(k1 + base), b = ldsr<f32x4>(k0 + base + 16) + ldsr<f32x4>(k1 + base + 16);
        const bool s0 = tid < 32;
        return (u32x4){pk(s0 ? b[3] : b[0], s0 ? b[2] : a[3]), pk(s0 ? b[1] : a[2], s0 ? b[0] : a[2]), pk(s0 ? a[3] : a[2], s0 ? a[2] : a[1]), pk(s0 ? a[1] : a[0], a[0])};
    } else if (w == 1) {
        const int base = (256 + 2 * tid) * 4;
        const f32x2 a = ldsr<f32x2>(k0 + base) + ldsr<f32x2>(k1 + base); const float b = ldsr<float>(k0 + base + 8) + ldsr<float>(k1 + base + 8);
        return (u32x4){pk(b, b), pk(a[1], a[1]), pk(a[1], a[0]), pk(a[0], a[0])};
    }
    const int base = (384 + tid) * 4;
    const float v0 = ldsr<float>(k0 + base) + ldsr<float>(k1 + base), v1 = ldsr<float>(k0 + base + 4) + ldsr<float>(k1 + base + 4);
    const unsigned lo = pk(v0, v0);
    return (u32x4){pk(v1, v1), pk(v1, (tid & 1) ? v1 : v0), lo, lo};
}
struct Frags { u32x4 F[8], G[8]; };
DI void frag_load(Frags& S, int i, LAS char* fp, LAS char* gp) {
    const u32x2 a = *(volatile LAS u32x2*)(fp + 512 * i), b = *(volatile LAS u32x2*)(fp + 512 * i + 8);
    S.F[i] = (u32x4){a[0], a[1], b[0], b[1]};
    S.G[i] = *(volatile LAS u32x4*)(gp + 512 * (7 - i));
}
DI void conv_step(f32x4 (&acc)[8], const Frags& C, Frags& N, LAS char* fp, LAS char* gp) {
#pragma unroll
    for (int t = 0; t < 8; ++t) {
        frag_load(N, t, fp, gp);
#pragma unroll
        for (int i = 0; i <= t; ++i) acc[t] = mfma16(C.F[i], C.G[t - i], acc[t]);
    }
#pragma unroll
    for (int k = 0; k < 24; ++k) { __builtin_amdgcn_sched_group_barrier(0x008, 1, 0); __builtin_amdgcn_sched_group_barrier(0x100, 1, 0); }
    __builtin_amdgcn_sched_group_barrier(0x008, 12, 0);
}
DI void store_y(const Ctx& X, unsigned off, int t, f32x4 v) {
    __builtin_amdgcn_raw_buffer_store_b64((u32x2){pk(v[0], v[1]), pk(v[2], v[3])}, X.rs, off, 512 * t, SC1);
}
DI void conv_job(const Ctx& X, const Modes& M, int ch0, int nch, int cg, unsigned myflag, unsigned utflag, int w, int l, LAS char* lds STAMP_ARG) {
    const int tid = threadIdx.x;
    const bool two = nch == 2;
    LAS char* kp = lds + 2 * nch * TABB;
    if (two || w < 2) kgen_tables(M, lds + w * TABB, l);
    STAMP(5);
    if (two) { DRAIN(); BAR(); if (tid == 0) publish(X, utflag); }
    if (two || w < 2) kgen_contract(lds + w * TABB, kp + w * 3072, l);
    STAMP(6);
    wait_flags(X, F_UT + (unsigned)(cg * 32 + (l & 31)) * 64);
    STAMP(7);
    u32x4 ua[2]; u32x2 ub[2];
    const unsigned uo = UTOFF + (unsigned)ch0 * 4096 + tid * 16;
    const unsigned uo2 = tid == 255 ? uo - 8 : uo;
#pragma unroll 1
    for (unsigned sp = 0; sp < SPIN_CAP; ++sp) {
        ua[0] = __builtin_amdgcn_raw_buffer_load_b128(X.rs, uo, 0, SC1); ub[0] = __builtin_amdgcn_raw_buffer_load_b64(X.rs, uo2 + 16, 0, SC1);
        ua[1] = ua[0]; ub[1] = ub[0];
        if (two) { ua[1] = __builtin_amdgcn_raw_buffer_load_b128(X.rs, uo, 4096, SC1); ub[1] = __builtin_amdgcn_raw_buffer_load_b64(X.rs, uo2 + 16, 4096, SC1); }
        if (!HINT_UT) break;
        if (__all(max(max(max4(ua[0]), max4(ua[1])), max(max(ub[0][0], ub[0][1]), max(ub[1][0], ub[1][1]))) != ~0u)) break;
        asm volatile("" ::: "memory");
    }
    BAR();
    u32x4 kx[2];
    kx[0] = kexpand(kp, w, tid); kx[1] = kx[0];
    if (two) kx[1] = kexpand(kp + 6144, w, tid);
    BAR();
    STAMP(8);
#pragma unroll
    for (int k = 0; k < 2; ++k) if (k == 0 || two) {
        LAS char* cb = lds + k * CHB;
        ldsw<u32x4>(cb + UCH + (2040 - 8 * tid) * 2, kx[k]);
        if (tid < 30) ldsw<u32x4>(cb + UCH + 4096 + 16 * tid, (u32x4){0, 0, 0, 0});
        const unsigned d0 = ua[k][0], d1 = ua[k][1], d2 = ua[k][2], d3 = ua[k][3], d4 = ub[k][0], d5 = ub[k][1];
        const unsigned a01 = __builtin_amdgcn_alignbit(d1, d0, 16), a12 = __builtin_amdgcn_alignbit(d2, d1, 16), a23 = __builtin_amdgcn_alignbit(d3, d2, 16),
                       a34 = __builtin_amdgcn_alignbit(d4, d3, 16), a45 = __builtin_amdgcn_alignbit(d5, d4, 16);
        LAS char* up = cb + 32 + 16 * tid;
        ldsw<u32x4>(up, ua[k]); ldsw<u32x4>(up + UCOPY, (u32x4){a01, a12, a23, a34});
        ldsw<u32x4>(up + 2 * UCOPY, (u32x4){d1, d2, d3, d4}); ldsw<u32x4>(up + 3 * UCOPY, (u32x4){a12, a23, a34, a45});
        if (w == 0) {
            const unsigned f0 = rfl(d0), f01 = rfl(a01);
            if (l < 8) {
                const int cc = l >> 1; const bool sec = l & 1;
                const unsigned e3 = !sec ? 0u : cc == 1 ? (f0 << 16) : cc == 2 ? f0 : cc == 3 ? f01 : 0u;
                const unsigned e2 = (sec && cc == 3) ? (f0 << 16) : 0u;
                ldsw<u32x4>(cb + cc * UCOPY + 16 * (l & 1), (u32x4){0, 0, e2, e3});
            }
        }
    }
    const int kch = two ? (w >> 1) : 0, res0 = two ? 4 * (w & 1) : 2 * w;
    const int r = l & 15, g = l >> 4, cp = (r + 1) & 3;
    LAS char* fp = lds + kch * CHB + cp * UCOPY + 2 * (25 + r - 8 * g - cp) + 64 * res0;
    LAS char* gp = lds + kch * CHB + UCH + 2 * (264 - 16 * r - 8 * g) + 64 * res0;
    unsigned yo = YTOFF + (unsigned)(ch0 + kch) * 4096 + (unsigned)(16 * r + 4 * g) * 2;
    LAS char* dp = lds + nch * CHB + l * 16;
    PIN(fp); PIN(gp); PIN(yo); PIN(dp);
    BAR();
    STAMP(9);
    f32x4 acc[8];
#pragma unroll
    for (int t = 0; t < 8; ++t) acc[t] = (f32x4){};
    Frags S0, S1;
#pragma unroll
    for (int i = 0; i < 8; ++i) frag_load(S0, i, fp, gp);
#pragma unroll 1
    for (int it = 0; it < nch; ++it) {
        conv_step(acc, S0, S1, fp + 64, gp + 64);
        conv_step(acc, S1, S0, fp + 128, gp + 128);
        fp += 128; gp += 128;
    }
    STAMP(10);
    if (two) {
        if (w & 1) {
#pragma unroll
            for (int t = 0; t < 4; ++t) ldsw<f32x4>(dp + (w * 4 + t) * 1024, acc[t]);
        } else {
#pragma unroll
            for (int t = 0; t < 4; ++t) ldsw<f32x4>(dp + (w * 4 + t) * 1024, acc[4 + t]);
        }
        BAR();
        LAS char* op = dp + (w ^ 1) * 4096;
        if (w & 1) {
#pragma unroll
            for (int t = 0; t < 4; ++t) store_y(X, yo, 4 + t, acc[4 + t] + ldsr<f32x4>(op + t * 1024));
        } else {
#pragma unroll
            for (int t = 0; t < 4; ++t) store_y(X, yo, t, acc[t] + ldsr<f32x4>(op + t * 1024));
        }
    } else {
#pragma unroll
        for (int t = 0; t < 8; ++t) ldsw<f32x4>(dp + (w * 8 + t) * 1024, acc[t]);
        BAR();
        LAS char* op = dp + w * 2048;
#pragma unroll
        for (int k = 0; k < 2; ++k)
            store_y(X, yo + 1024 * w, k, (ldsr<f32x4>(op + k * 1024) + ldsr<f32x4>(op + (8 + k) * 1024)) + (ldsr<f32x4>(op + (16 + k) * 1024) + ldsr<f32x4>(op + (24 + k) * 1024)));
    }
    STAMP(11);
    DRAIN(); BAR();
    if (tid == 0) publish(X, myflag);
}
DI void phase_t2(const Params& P, const Ctx& X, int cg, int lb, int w, int l, LAS char* lds, u32x4 (&ur)[3], const u32x4 (&dv)[3] STAMP_ARG) {
    const int tid = threadIdx.x, c = l & 15, g = l >> 4, q = c >> 2, pp = c & 3;
#pragma unroll
    for (int m = 0; m < 3; ++m) {
#pragma unroll
        for (int i = 0; i < 4; ++i) ur[m][i] = pk(bflo(ur[m][i]) * bflo(dv[m][i]), bfhi(ur[m][i]) * bfhi(dv[m][i]));
        PIN(ur[m]);
    }
    unsigned go = YTOFF + (unsigned)(72 * cg + (tid >> 3)) * 4096 + (unsigned)(128 * lb + 16 * (tid & 7));
    unsigned go2 = YTOFF + (unsigned)(72 * cg + 64 + (l >> 3)) * 4096 + (unsigned)(128 * lb + 16 * (tid & 7));
    LAS char* wp = lds + (tid >> 3) * 144 + 16 * (tid & 7);
    LAS char* rp = lds + (8 * g + q) * 144 + 32 * w + 8 * pp;
    LAS char* rp2 = lds + (64 + q) * 144 + 32 * w + 8 * pp;
    unsigned oo = (unsigned)(64 * lb + 16 * w + c) * 2304 + cg * 144 + 16 * g;
    PIN(go); PIN(go2); PIN(wp); PIN(rp); PIN(rp2); PIN(oo);
    wait_flags(X, F_YT + (unsigned)(l < 32 ? cg * 32 + l : 256 + cg * 8 + (l & 7)) * 64);
    STAMP(12);
    u32x4 yv[3];
#pragma unroll 1
    for (unsigned sp = 0; sp < SPIN_CAP; ++sp) {
        yv[0] = __builtin_amdgcn_raw_buffer_load_b128(X.rs, go, 0, SC1); yv[1] = __builtin_amdgcn_raw_buffer_load_b128(X.rs, go, 32 * 4096, SC1);
        yv[2] = yv[0];
        if (w == 0) yv[2] = __builtin_amdgcn_raw_buffer_load_b128(X.rs, go2, 0, SC1);
        if (!HINT_YT) break;
        if (__all(max(max(max4(yv[0]), max4(yv[1])), max4(yv[2])) != ~0u)) break;
        asm volatile("" ::: "memory");
    }
    ldsw<u32x4>(wp, yv[0]); ldsw<u32x4>(wp + 32 * 144, yv[1]); if (w == 0) ldsw<u32x4>(wp + 64 * 144, yv[2]);
    BAR();
    STAMP(13);
    const rsrc_t ro = __builtin_amdgcn_make_buffer_rsrc((void*)P.out, 0, 2048 * 2304, 0x00020000);
#pragma unroll
    for (int m = 0; m < 3; ++m) {
        LAS char* p = m < 2 ? rp + 32 * 144 * m : rp2;
        const u32x2 lo = trread(p), hi = trread(p + 4 * 144);
        const u32x4 y = {lo[0], lo[1], hi[0], hi[1]};
        u32x4 r;
#pragma unroll
        for (int i = 0; i < 4; ++i) r[i] = pk(bflo(y[i]) + bflo(ur[m][i]), bfhi(y[i]) + bfhi(ur[m][i]));
        if (m < 2 || g == 0) __builtin_amdgcn_raw_buffer_store_b128(r, ro, oo + 64 * m, 0, 0);
    }
    STAMP(14);
}

DI Ctx make_ctx(const Params& P) {
    const u64 e = __hip_atomic_load((GAS u64*)(P.ws + EPOCH_OFF), RLX_AGENT);
    const unsigned lo = rfl((unsigned)e), hi = rfl((unsigned)(e >> 32));
    Ctx X;
    X.tag = (((u64)hi << 32) | lo) + 0x9E3779B97F4A7C15ull;
    char* base = P.ws + (size_t)((unsigned)X.tag % NSLOT) * SLOTB;
    X.slot = (GAS char*)base;
    X.rs = __builtin_amdgcn_make_buffer_rsrc(base, 0, SLOTB, 0x00020000);
    return X;
}

__global__ void __launch_bounds__(256, 2) fused_kernel(Params P) {
    __shared__ __attribute__((aligned(16))) char smem[LDS_BYTES];
    LAS char* lds = (LAS char*)smem;
    const int b = blockIdx.x, tid = threadIdx.x, l = tid & 63, w = rfl(tid >> 6);
    STAMP(0);
    const bool roleB = b < 256;
    const int s2 = (b & 7) * 8 + ((b >> 3) & 7), j = (b >> 6) & 3, cg = b & 7, lb = (b >> 3) & 31;
    const bool extra = roleB && b >= 192;
    const int ch0 = roleB ? 72 * cg + 64 + ((b >> 3) & 7) : 72 * cg + 2 * lb;
    u32x4 ur[3], dv[3];
    Modes M = {};
    Ctx X;
    if (roleB) {
        __builtin_amdgcn_s_setprio(2);
        p1_issue(P, s2, w, l, lds);
        X = make_ctx(P);
        if (extra) M = load_modes(P, ch0, w & 1, l);
        p1_tables(s2, j, w, l, lds);
        phase_p1(X, s2, j, w, l, lds STAMP_PASS);
        DRAIN();
        if (l == 0) publish(X, F_E1 + (unsigned)((4 * j + w) * 64 + s2) * 64);
        BAR();
        if (b < 145) phase_p2(P, X, b, w, l, lds STAMP_PASS);
        else if (b == 160 && w == 0) {
#pragma unroll 1
            for (int k = 0; k < 4; ++k) { wait_flags(X, F_UT + (unsigned)(64 * k + l) * 64); wait_flags(X, F_E1 + (unsigned)(256 * k + l) * 64); }
            if (l == 0) __hip_atomic_store((GAS u64*)(P.ws + EPOCH_OFF), X.tag, RLX_AGENT);
        }
    } else {
        const int c = l & 15, g = l >> 4;
        const GAS char* src = (const GAS char*)P.u + (size_t)(64 * lb + 16 * w + c) * 1152 + cg * 144;
        ur[0] = *(const GAS u32x4*)(src + 16 * g); ur[1] = *(const GAS u32x4*)(src + 64 + 16 * g); ur[2] = *(const GAS u32x4*)(src + 128);
        M = load_modes(P, ch0 + (w >> 1), w & 1, l);
        const GAS char* dsrc = (const GAS char*)P.Dskip + cg * 144;
        dv[0] = *(const GAS u32x4*)(dsrc + 16 * g); dv[1] = *(const GAS u32x4*)(dsrc + 64 + 16 * g); dv[2] = *(const GAS u32x4*)(dsrc + 128);
        X = make_ctx(P);
        STAMP(1);
        phase_t1(X, cg, lb, w, l, lds, ur STAMP_PASS);
        STAMP(2);
    }
    if (!roleB || extra)
        conv_job(X, M, ch0, roleB ? 1 : 2, cg, F_YT + (unsigned)(roleB ? 256 + cg * 8 + ((b >> 3) & 7) : cg * 32 + lb) * 64, F_UT + (unsigned)(cg * 32 + lb) * 64, w, l, lds STAMP_PASS);
    if (roleB) phase_p3(P, X, s2, j, w, l, lds STAMP_PASS);
    else phase_t2(P, X, cg, lb, w, l, lds, ur, dv STAMP_PASS);
}

extern "C" void kernel_launch(void* const* d_in, const int* in_sizes, int n_in, void* d_out, int out_size, void* d_ws, size_t ws_size, hipStream_t stream) {
    Params P;
    P.u = (const u16*)d_in[0]; P.logA = (const u16*)d_in[1]; P.Aim = (const u16*)d_in[2]; P.Cre = (const u16*)d_in[3]; P.Cim = (const u16*)d_in[4];
    P.logdt = (const u16*)d_in[5]; P.Dskip = (const u16*)d_in[6]; P.x = (const u16*)d_in[7]; P.wr = (const u16*)d_in[8]; P.wi = (const u16*)d_in[9];
    P.out = (u16*)d_out; P.ws = (char*)d_ws;
    fused_kernel<<<512, 256, 0, stream>>>(P);
}
```

```cpp
#include <hip/hip_runtime.h>
#include <hip/hip_bf16.h>
#include <stdint.h>

#define HINT_E1 0
#define HINT_E2 0
#define HINT_UT 0
#define HINT_YT 0
#define NSLOT 1

#define GAS __attribute__((address_space(1)))
#define LAS __attribute__((address_space(3)))
#define DI __device__ __forceinline__
typedef __bf16 bf16x8 __attribute__((ext_vector_type(8)));
typedef __bf16 bf16x2 __attribute__((ext_vector_type(2)));
typedef float f32x2 __attribute__((ext_vector_type(2)));
typedef float f32x4 __attribute__((ext_vector_type(4)));
typedef float f32x16 __attribute__((ext_vector_type(16)));
typedef unsigned u32x2 __attribute__((ext_vector_type(2)));
typedef unsigned u32x4 __attribute__((ext_vector_type(4)));
typedef short s16x4 __attribute__((ext_vector_type(4)));
typedef unsigned short u16;
typedef unsigned long long u64;
typedef __amdgpu_buffer_rsrc_t rsrc_t;
#define RLX_AGENT __ATOMIC_RELAXED, __HIP_MEMORY_SCOPE_AGENT
#define PIN(x) asm volatile("" : "+v"(x))
#define BAR() asm volatile("s_waitcnt lgkmcnt(0)\n\ts_barrier" ::: "memory")
#define DRAIN() asm volatile("s_waitcnt vmcnt(0)" ::: "memory")
constexpr int SC1 = 16;

DI unsigned pk(float a, float b) { f32x2 v = {a, b}; return __builtin_bit_cast(unsigned, __builtin_convertvector(v, bf16x2)); }
DI unsigned pk2(f32x2 v) { return __builtin_bit_cast(unsigned, __builtin_convertvector(v, bf16x2)); }
DI float bflo(unsigned v) { return __builtin_bit_cast(float, v << 16); }
DI float bfhi(unsigned v) { return __builtin_bit_cast(float, v & 0xffff0000u); }
DI float bfround(float x) { return bflo(pk(x, x)); }
DI f32x4 mfma16(u32x4 a, u32x4 b, f32x4 c) { return __builtin_amdgcn_mfma_f32_16x16x32_bf16(__builtin_bit_cast(bf16x8, a), __builtin_bit_cast(bf16x8, b), c, 0, 0, 0); }
DI f32x16 mfma32(u32x4 a, u32x4 b, f32x16 c) { return __builtin_amdgcn_mfma_f32_32x32x16_bf16(__builtin_bit_cast(bf16x8, a), __builtin_bit_cast(bf16x8, b), c, 0, 0, 0); }
DI float cosr(float x) { return __builtin_amdgcn_cosf(x); }
DI float sinr(float x) { return __builtin_amdgcn_sinf(x); }
DI u32x2 trread(LAS char* p) { return __builtin_bit_cast(u32x2, __builtin_amdgcn_ds_read_tr16_b64_v4i16((LAS s16x4*)p)); }
template <class T> DI T ldsr(LAS char* p) { return *(LAS T*)p; }
template <class T> DI void ldsw(LAS char* p, T v) { *(LAS T*)p = v; }
DI int rfl(int x) { return __builtin_amdgcn_readfirstlane(x); }
DI f32x2 cmul(f32x2 a, f32x2 b) { const f32x2 t = a * b.xx; return __builtin_elementwise_fma(a.yx, (f32x2){-b.y, b.y}, t); }
DI unsigned max4(u32x4 v) { return max(max(v[0], v[1]), max(v[2], v[3])); }

struct Cos1152 { float v[1152]; };
constexpr Cos1152 make_cos() {
    Cos1152 t{};
    for (int k = 0; k < 1152; ++k) {
        const double x = 6.283185307179586476925 * (double)(k <= 576 ? k : k - 1152) / 1152.0, x2 = x * x;
        double term = 1.0, sum = 1.0;
        for (int n = 1; n < 18; ++n) { term *= -x2 / (double)((2 * n - 1) * (2 * n)); sum += term; }
        t.v[k] = (float)((sum < 1e-12 && sum > -1e-12) ? 0.0 : sum);
    }
    return t;
}
constexpr Cos1152 COS = make_cos();
constexpr float ccos(int k) { return COS.v[k % 1152]; }
constexpr float csin(int k) { return COS.v[(k + 864) % 1152]; }
constexpr unsigned cbits(float f) { return __builtin_bit_cast(unsigned, f); }
constexpr unsigned cbf(float f) { const unsigned u = cbits(f); return (u + 0x7fffu + ((u >> 16) & 1u)) >> 16; }
constexpr unsigned cpk(float a, float b) { return cbf(a) | (cbf(b) << 16); }
struct Tab12 { unsigned v[12 * 256]; };
struct Tab16 { unsigned v[16 * 256]; };
constexpr Tab12 make_t576(bool spec) {
    Tab12 t{};
    for (int s = 0; s < 3; ++s) for (int l = 0; l < 64; ++l) for (int i = 0; i < 4; ++i) {
        const int n = l & 31, idx = 8 * s + 4 * (l >> 5) + i;
        const bool hi = (n >> 3) & 1;
        const int ka = !spec ? n : (n & 7) + 8 * (n >> 4);
        const float c = ccos(48 * idx * ka), sn = csin(48 * idx * ka);
        t.v[s * 256 + l * 4 + i] = !spec ? cpk(c, sn) : hi ? cpk(0.f, c) : cpk(c, 0.f);
        t.v[(3 + s) * 256 + l * 4 + i] = !spec ? cpk(-sn, c) : hi ? cpk(0.f, -sn) : cpk(-sn, 0.f);
        t.v[(6 + s) * 256 + l * 4 + i] = cbits(ccos(2 * idx * ka));
        t.v[(9 + s) * 256 + l * 4 + i] = cbits(csin(2 * idx * ka));
    }
    return t;
}
constexpr Tab16 make_t64(bool half) {
    Tab16 t{};
    for (int q = 0; q < 4; ++q) for (int l = 0; l < 64; ++l) for (int i = 0; i < 4; ++i) {
        const int c = l & 15, u = 4 * (l >> 4) + i;
        const int a1 = 18 * c * (q + 4 * u) + (half ? 9 * (q + 4 * u) : 0), a2 = 18 * u * (q + 4 * c) + (half ? 9 * (q + 4 * c) : 0);
        t.v[q * 256 + l * 4 + i] = cpk(ccos(a1), csin(a1)); t.v[(4 + q) * 256 + l * 4 + i] = cpk(-csin(a1), ccos(a1));
        t.v[(8 + q) * 256 + l * 4 + i] = cpk(ccos(a2), csin(a2)); t.v[(12 + q) * 256 + l * 4 + i] = cpk(csin(a2), -ccos(a2));
    }
    return t;
}
struct Tables { Tab12 n576, s576; Tab16 n64, h64; };
static __device__ const __attribute__((aligned(256))) Tables TABLES = {make_t576(false), make_t576(true), make_t64(false), make_t64(true)};
constexpr unsigned TO_N576 = 0, TO_S576 = 12288, TO_N64 = 24576, TABLE_BYTES = 57344;

struct Params {
    const u16 *u, *logA, *Aim, *Cre, *Cim, *logdt, *Dskip, *x, *wr, *wi;
    u16* out; char* ws;
};

constexpr unsigned ROWB = 3072;
constexpr unsigned SLABG = 37888;
constexpr unsigned E1OFF = 0, E2OFF = 1024 * ROWB, UTOFF = E2OFF + 64 * SLABG + 4096, YTOFF = UTOFF + 576 * 4096 + 4096, FLOFF = YTOFF + 576 * 4096;
constexpr unsigned F_E1 = FLOFF, F_E2 = F_E1 + 1024 * 64, F_UT = F_E2 + 256 * 64, F_YT = F_UT + 256 * 64, SLOT_USED = F_YT + 320 * 64;
constexpr unsigned SLOTB = 11u << 20, EPOCH_OFF = 200u << 20;
static_assert(SLOT_USED <= SLOTB, "slot");
constexpr int B_XM = 0, B_XT = 32768, B_SPEC = 36864, B_SLAB = 0, B_P2T = 0, B_ZI = 55744, IMGB = 2704, B_TAB = 66560;
constexpr int T_M1 = B_TAB + 12288, T_M3 = B_TAB + 13312, L_JUNK = 53504, L_FAIL = 55552, B_WIMG = 32768, WROW = 160;
constexpr int C_U2 = 4224, C_K0 = 8448, C_K1 = C_K0 + 4576, CHB = C_K1 + 4576, TABB = 53 * 272, A_STG = 69952;
constexpr int LDS_BYTES = 81920;
constexpr unsigned SPIN_CAP = 1u << 18;
constexpr u32x4 INVALID = {~0u, ~0u, ~0u, ~0u};
#define CODE_PF 16384

#define STAMP(k)
#define STAMP_ARG
#define STAMP_PASS
#define VMWAIT(n) asm volatile("s_waitcnt vmcnt(" #n ")" ::: "memory")
#define PRIO(n) __builtin_amdgcn_s_setprio(n)

struct Ctx { rsrc_t rs; GAS char* slot; u64 tag; };
DI u32x4 make_srd(const void* p, unsigned bytes) { const u64 a = (u64)p; return (u32x4){(unsigned)a, (unsigned)(a >> 32) & 0xffffu, bytes, 0x00020000u}; }
DI void dma16(u32x4 srd, unsigned voff, unsigned soff, unsigned dst) {
    asm volatile("s_mov_b32 m0, %3\n\ts_nop 0\n\tbuffer_load_dwordx4 %0, %1, %2 offen lds" :: "v"(voff), "s"(srd), "s"(soff), "s"(dst) : "memory");
}
DI void dma16x3(u32x4 srd, unsigned voff, unsigned soff, unsigned dst) {
    asm volatile("s_mov_b32 m0, %3\n\ts_nop 0\n\tbuffer_load_dwordx4 %0, %1, %2 offen lds\n\tbuffer_load_dwordx4 %0, %1, %2 offen offset:1024 lds\n\tbuffer_load_dwordx4 %0, %1, %2 offen offset:2048 lds"
                 :: "v"(voff), "s"(srd), "s"(soff), "s"(dst) : "memory");
}
DI void dma16x4(u32x4 srd, unsigned voff, unsigned soff, unsigned dst) {
    asm volatile("s_mov_b32 m0, %3\n\ts_nop 0\n\tbuffer_load_dwordx4 %0, %1, %2 offen lds\n\tbuffer_load_dwordx4 %0, %1, %2 offen offset:1024 lds\n\t"
                 "buffer_load_dwordx4 %0, %1, %2 offen offset:2048 lds\n\tbuffer_load_dwordx4 %0, %1, %2 offen offset:3072 lds" :: "v"(voff), "s"(srd), "s"(soff), "s"(dst) : "memory");
}
DI void dma16x3_sc1(u32x4 srd, unsigned voff, unsigned soff, unsigned dst) {
    asm volatile("s_mov_b32 m0, %3\n\ts_nop 0\n\tbuffer_load_dwordx4 %0, %1, %2 offen sc1 lds\n\tbuffer_load_dwordx4 %0, %1, %2 offen offset:1024 sc1 lds\n\tbuffer_load_dwordx4 %0, %1, %2 offen offset:2048 sc1 lds"
                 :: "v"(voff), "s"(srd), "s"(soff), "s"(dst) : "memory");
}
DI void dma16x4_sc1(u32x4 srd, unsigned voff, unsigned soff, unsigned dst) {
    asm volatile("s_mov_b32 m0, %3\n\ts_nop 0\n\tbuffer_load_dwordx4 %0, %1, %2 offen sc1 lds\n\tbuffer_load_dwordx4 %0, %1, %2 offen offset:1024 sc1 lds\n\t"
                 "buffer_load_dwordx4 %0, %1, %2 offen offset:2048 sc1 lds\n\tbuffer_load_dwordx4 %0, %1, %2 offen offset:3072 sc1 lds" :: "v"(voff), "s"(srd), "s"(soff), "s"(dst) : "memory");
}
DI void dma16_sc1(u32x4 srd, unsigned voff, unsigned soff, unsigned dst) {
    asm volatile("s_mov_b32 m0, %3\n\ts_nop 0\n\tbuffer_load_dwordx4 %0, %1, %2 offen sc1 lds" :: "v"(voff), "s"(srd), "s"(soff), "s"(dst) : "memory");
}
DI void touch(u32x4 srd, unsigned voff, unsigned soff, unsigned dst) {
    asm volatile("s_nop 4\n\ts_mov_b32 m0, %3\n\ts_nop 0\n\tbuffer_load_dword %0, %1, %2 offen lds" :: "v"(voff), "s"(srd), "s"(soff), "s"(dst) : "memory");
}

DI void publish(const Ctx& X, unsigned off) { __hip_atomic_store((GAS u64*)(X.slot + off), X.tag, RLX_AGENT); }
DI void wait_flags(const Ctx& X, unsigned off) {
#pragma unroll 1
    for (unsigned sp = 0; sp < SPIN_CAP; ++sp) {
        const u64 f = __hip_atomic_load((GAS u64*)(X.slot + off), RLX_AGENT);
        if (__all(f == X.tag)) break;
        __builtin_amdgcn_s_sleep(1);
    }
    __builtin_amdgcn_fence(__ATOMIC_ACQUIRE, "wavefront");
}
DI void wait_flags3(const Ctx& X, unsigned o0, unsigned o1, unsigned o2) {
#pragma unroll 1
    for (unsigned sp = 0; sp < SPIN_CAP; ++sp) {
        const u64 f0 = __hip_atomic_load((GAS u64*)(X.slot + o0), RLX_AGENT), f1 = __hip_atomic_load((GAS u64*)(X.slot + o1), RLX_AGENT), f2 = __hip_atomic_load((GAS u64*)(X.slot + o2), RLX_AGENT);
        if (__all(f0 == X.tag && f1 == X.tag && f2 == X.tag)) break;
        __builtin_amdgcn_s_sleep(1);
    }
    __builtin_amdgcn_fence(__ATOMIC_ACQUIRE, "wavefront");
}
DI void epoch_issue(const Params& P, int l, unsigned dst) {
    if (l < 2) asm volatile("s_mov_b32 m0, %2\n\ts_nop 0\n\tbuffer_load_dword %0, %1, 0 offen sc1 lds" :: "v"(l * 4), "s"(make_srd(P.ws + EPOCH_OFF, 8)), "s"(dst) : "memory");
}
DI u32x4 slot_srd(GAS char* slot) {
    u32x4 srd = {(unsigned)rfl((unsigned)(u64)slot), (unsigned)rfl((unsigned)((u64)slot >> 32)) & 0xffffu, SLOTB, 0x00020000u};
    asm volatile("s_nop 4" : "+s"(srd));
    return srd;
}
DI Ctx make_ctx(const Params& P, LAS char* ep) {
    Ctx X;
    const u32x2 e = ldsr<u32x2>(ep);
    X.tag = (((u64)(unsigned)rfl(e[1]) << 32) | (unsigned)rfl(e[0])) + 0x9E3779B97F4A7C15ull;
    char* base = P.ws + (size_t)((unsigned)X.tag % NSLOT) * SLOTB;
    X.slot = (GAS char*)base;
    X.rs = __builtin_amdgcn_make_buffer_rsrc(base, 0, SLOTB, 0x00020000);
    return X;
}

struct DftC { u32x4 P1[3], Q1[3], P2[3], Q2[3]; f32x4 tc[3], ts[3]; };
DI void dft576(const u32x4 (&z)[3], const DftC& C, f32x16& oR, f32x16& oI) {
    f32x16 aR = {}, aI = {};
#pragma unroll
    for (int s = 0; s < 3; ++s) { aR = mfma32(z[s], C.P1[s], aR); aI = mfma32(z[s], C.Q1[s], aI); }
    oR = (f32x16){}; oI = (f32x16){};
#pragma unroll
    for (int s = 0; s < 3; ++s) {
        u32x4 y;
#pragma unroll
        for (int k = 0; k < 2; ++k) {
            const f32x2 r = {aR[4 * s + 2 * k], aR[4 * s + 2 * k + 1]}, i = {aI[4 * s + 2 * k], aI[4 * s + 2 * k + 1]};
            const f32x2 c = {C.tc[s][2 * k], C.tc[s][2 * k + 1]}, sn = {C.ts[s][2 * k], C.ts[s][2 * k + 1]};
            const f32x2 yr = __builtin_elementwise_fma(i, sn, r * c), yi = __builtin_elementwise_fma(-r, sn, i * c);
            y[2 * k] = pk(yr[0], yi[0]); y[2 * k + 1] = pk(yr[1], yi[1]);
        }
        oR = mfma32(y, C.P2[s], oR); oI = mfma32(y, C.Q2[s], oI);
    }
}

DI void p1_issue(const Params& P, u32x4 tsrd, int s2, int j, int w, int l) {
    const u32x4 xs = make_srd(P.x, 2048 * 1152);
    const unsigned so = (unsigned)(512 * w + s2) * 1152, l16 = l * 16;
    const int rr = l >> 3;
    const unsigned vt = rr * (64 * 1152) + 1024 + (((l & 7) ^ (rr & 6)) * 16);
    unsigned sc, vc;
#define XROW(k) "s_add_u32 m0, m0, 0x400\n\ts_add_u32 %0, %0, 0x12000\n\tv_xor_b32 %1, " #k ", %2\n\tbuffer_load_dwordx4 %1, %3, %0 offen lds\n\t"
    asm volatile("s_mov_b32 m0, %5\n\ts_mov_b32 %0, %4\n\tbuffer_load_dwordx4 %2, %3, %4 offen lds\n\t"
                 XROW(32) XROW(64) XROW(0x60) XROW(0x80) XROW(0xa0) XROW(0xc0) XROW(0xe0)
                 "s_mov_b32 m0, %6\n\ts_nop 0\n\tbuffer_load_dwordx4 %7, %3, %4 offen lds"
                 : "=&s"(sc), "=&v"(vc) : "v"(l16), "s"(xs), "s"(so), "s"(B_XM + 8192 * w), "s"(B_XT + 1024 * w), "v"(vt) : "memory", "scc");
    dma16x3(tsrd, l16, TO_N576 + w * 3072, B_TAB + w * 3072);
    if (j == 0) dma16x3(tsrd, l16, TO_S576 + w * 3072, B_SPEC + w * 3072);
}
DI void phase_p1(const Params& P, Ctx& X, LAS char* ep, int s2, int j, int w, int l, LAS char* lds STAMP_ARG) {
    const int c = l & 15, g = l >> 4, q = c >> 2, pp = c & 3, r = 4 * g + q;
    {
        const int i = c & 3, kap = 4 * j + (c >> 2), s1 = (w < 2 ? 0 : 12) + 4 * g + 2 * w;
        const float qq = i == 1 ? 0.25f : 0.f;
        float a0 = (float)(((64 * s1 + s2) * kap) & 2047) * (1.0f / 2048.0f) + qq, a1 = (float)(((64 * s1 + 64 + s2) * kap) & 2047) * (1.0f / 2048.0f) + qq;
        if (kap == 0 && i == 1) { a0 = 0.f; a1 = 0.5f; }
        ldsw<unsigned>(lds + T_M1 + l * 16 + w * 4, i < 2 ? pk(cosr(a0), cosr(a1)) : 0u);
    }
    LAS char* x0 = lds + B_XM + r * 1024 + (2 * (w ^ q) + (pp >> 1)) * 16 + 8 * (pp & 1);
    LAS char* xe = x0 + 128 * (g & 1); LAS char* xo = x0 - 128 * (g & 1);
    LAS char* xt = lds + B_XT + r * 128 + ((2 * w + (pp >> 1)) ^ (r & 6)) * 16 + 8 * (pp & 1);
    LAS char* za[3];
#pragma unroll
    for (int m = 0; m < 3; ++m) { const unsigned d = 64 * m + 16 * w + c, a = (d * 2731u) >> 16; za[m] = lds + B_ZI + g * IMGB + (112 * d - 2684 * a); PIN(za[m]); }
    const bool special = (j == 0 && w == 0);
    const int kb = l & 31, h = l >> 5;
    LAS char* zr = lds + B_ZI + w * IMGB + ((kb < 24 ? kb : 23) * 28 + 4 * h) * 4;
    LAS char* t2 = lds + B_TAB + l * 16; LAS char* t1 = special ? lds + B_SPEC + l * 16 : t2;
    const unsigned rowoff = E1OFF + (unsigned)((4 * j + w) * 64 + s2) * ROWB;
    unsigned so = rowoff + (unsigned)(kb * 8 + h * 4) * 4;
    PIN(xe); PIN(xo); PIN(xt); PIN(zr); PIN(so); PIN(t1); PIN(t2);
    STAMP(1);
    constexpr int NC = HINT_E1 ? 3 : 0;
    if (j == 0) asm volatile("s_waitcnt vmcnt(%0)" :: "i"(11 + NC) : "memory");
    else asm volatile("s_waitcnt vmcnt(%0)" :: "i"(3 + NC) : "memory");
    BAR();
    STAMP(2);
    {
        const u32x4 M1 = ldsr<u32x4>(lds + T_M1 + l * 16);
        u32x2 lo[9], hi[9];
#pragma unroll
        for (int m = 0; m < 9; ++m) { LAS char* p = m == 8 ? xt : ((m & 1) ? xo : xe) + 128 * m; lo[m] = trread(p); hi[m] = trread(p + (m == 8 ? 2048 : 16384)); }
        f32x4 d[9];
#pragma unroll
        for (int m = 0; m < 9; ++m) d[m] = mfma16(M1, (u32x4){lo[m][0], lo[m][1], hi[m][0], hi[m][1]}, (f32x4){});
#pragma unroll
        for (int m = 0; m < 9; ++m) ldsw<unsigned>(za[m % 3] + 32 * (m / 3), pk(d[m][0], d[m][1]));
        __builtin_amdgcn_sched_group_barrier(0x100, 19, 0);
#pragma unroll
        for (int m = 0; m < 3; ++m) __builtin_amdgcn_sched_group_barrier(0x008, 1, 0);
#pragma unroll
        for (int m = 0; m < 6; ++m) { __builtin_amdgcn_sched_group_barrier(0x008, 1, 0); __builtin_amdgcn_sched_group_barrier(0x002, 1, 0); __builtin_amdgcn_sched_group_barrier(0x200, 1, 0); }
    }
    X = make_ctx(P, ep);
    VMWAIT(0);
    BAR();
    STAMP(3);
    u32x4 z[3]; DftC C;
#pragma unroll
    for (int s = 0; s < 3; ++s) { z[s] = ldsr<u32x4>(zr + 32 * s); C.P1[s] = ldsr<u32x4>(t1 + s * 1024); C.Q1[s] = ldsr<u32x4>(t1 + (3 + s) * 1024); }
#pragma unroll
    for (int s = 0; s < 3; ++s) { C.tc[s] = ldsr<f32x4>(t1 + (6 + s) * 1024); C.ts[s] = ldsr<f32x4>(t1 + (9 + s) * 1024); }
    if (special) {
#pragma unroll
        for (int s = 0; s < 3; ++s) { C.P2[s] = ldsr<u32x4>(t2 + s * 1024); C.Q2[s] = ldsr<u32x4>(t2 + (3 + s) * 1024); }
    } else {
#pragma unroll
        for (int s = 0; s < 3; ++s) { C.P2[s] = C.P1[s]; C.Q2[s] = C.Q1[s]; }
    }
    f32x16 oR, oI;
    dft576(z, C, oR, oI);
    if (kb < 24) {
#pragma unroll
        for (int i = 0; i < 3; ++i) {
            const u32x4 v = {pk(oR[4 * i], oI[4 * i]), pk(oR[4 * i + 1], oI[4 * i + 1]), pk(oR[4 * i + 2], oI[4 * i + 2]), pk(oR[4 * i + 3], oI[4 * i + 3])};
            __builtin_amdgcn_raw_buffer_store_b128(v, X.rs, so + 768 * i, 0, SC1);
        }
        if (special) __builtin_amdgcn_raw_buffer_store_b128((u32x4){pk(oR[12], oI[12]), pk(oR[13], oI[13]), pk(oR[14], oI[14]), pk(oR[15], oI[15])}, X.rs, so + 2304, 0, SC1);
    }
    STAMP(4);
}

DI void radix4(f32x4 (&R)[4], f32x4 (&I)[4]) {
    const f32x4 aR = R[0] + R[2], aI = I[0] + I[2], bR = R[0] - R[2], bI = I[0] - I[2];
    const f32x4 cR = R[1] + R[3], cI = I[1] + I[3], dR = R[1] - R[3], dI = I[1] - I[3];
    R[0] = aR + cR; I[0] = aI + cI; R[2] = aR - cR; I[2] = aI - cI;
    R[1] = bR + dI; I[1] = bI - dR; R[3] = bR - dI; I[3] = bI + dR;
}
struct P2Geom { int row, blk, oct, kap, ko, idx; bool owner; };
DI P2Geom p2_geom(int b) {
    const int k = (b >> 3) - 8, k3 = k / 3, i = k - 3 * k3, U = (b & 7) + 8 * k3;
    P2Geom G;
    G.owner = b >= 64 && (U < 45 || (U < 51 && i < 2));
    if (U < 45) { const int r = U / 3; G.row = r + 1; G.oct = U - 3 * r; G.blk = i; G.kap = r + 1; G.ko = 8 * i; G.idx = 3 * U + i; }
    else { const int cls = (U - 45) & 1; G.row = 0; G.oct = (U - 45) >> 1; G.blk = 2 * i + cls; G.kap = 16 * cls; G.ko = 8 * i; G.idx = 135 + 2 * (U - 45) + i; }
    return G;
}
DI unsigned e2_rowoff(int row) { return row ? 768u + 2304u * row + 64u * (row >> 2) : 0u; }
typedef u32x4 u32x4_u __attribute__((aligned(2)));
struct WRegs { u32x4 r[2], i[2]; };
DI void w_issue(const Params& P, const P2Geom& G, int w, int l, WRegs& W) {
    const int kb = 8 * G.oct + (l & 7), ks = G.kap + 32 * (16 * w + (l >> 3));
    const bool mir = kb >= 12;
    const int kd0 = 24 * kb + G.ko;
    const unsigned o0 = (mir ? ((2048 - ks) & 2047) * 289 + 569 - kd0 : ks * 289 + kd0) * 2;
    const unsigned o1 = (mir ? (1792 - ks) * 289 + 569 - kd0 : (ks + 256) * 289 + kd0) * 2;
    W.r[0] = *(const GAS u32x4_u*)((const GAS char*)P.wr + o0); W.i[0] = *(const GAS u32x4_u*)((const GAS char*)P.wi + o0);
    W.r[1] = *(const GAS u32x4_u*)((const GAS char*)P.wr + o1); W.i[1] = *(const GAS u32x4_u*)((const GAS char*)P.wi + o1);
}
DI void phase_p2(const Params& P, const Ctx& X, const P2Geom& G, u32x4 tsrd, unsigned myflag, int w, int l, LAS char* lds STAMP_ARG) {
    WRegs W; w_issue(P, G, w, l, W);
    dma16x4(tsrd, l * 16, TO_N64 + (G.kap == 16 ? 16384 : 0) + w * 4096, B_P2T + w * 4096);
    VMWAIT(8);
    if (l == 0) publish(X, myflag);
    const int c = l & 15, g = l >> 4, row = G.row, pos0 = 192 * G.blk + 64 * G.oct + 16 * w, p = pos0 + c;
    {
        const bool mir = 8 * G.oct + (l & 7) >= 12;
        const unsigned sel = mir ? 0x05040706u : 0x03020100u;
        u32x4 v[4];
#pragma unroll
        for (int k = 0; k < 4; ++k) {
            const u32x4 t = k < 2 ? W.r[k] : W.i[k - 2];
#pragma unroll
            for (int e = 0; e < 4; ++e) v[k][e] = __builtin_amdgcn_perm(t[3 - e], t[e], sel);
        }
        if (G.ko == 0 && G.oct < 2) {
            if ((l & 7) == 4 * G.oct) {
#pragma unroll
                for (int q = 0; q < 2; ++q) {
                    const int ks = G.kap + 32 * (16 * w + 8 * q + (l >> 3));
                    const int o = (mir ? ks : ((2048 - ks) & 2047)) * 289 + 288 * G.oct;
                    const float r2 = bflo(((const GAS u16*)P.wr)[o]), i2 = bflo(((const GAS u16*)P.wi)[o]);
                    const unsigned e = pk(0.5f * (bflo(v[q][0]) + r2), 0.5f * (bflo(v[2 + q][0]) - i2));
                    v[q][0] = (v[q][0] & 0xffff0000u) | (e & 0xffffu); v[2 + q][0] = (v[2 + q][0] & 0xffff0000u) | (e >> 16);
                }
            }
        }
        LAS char* wp = lds + B_WIMG + (16 * w + (l >> 3)) * WROW + 16 * (l & 7);
#pragma unroll
        for (int k = 0; k < 4; ++k) ldsw<u32x4>(wp + (k & 1) * 8 * WROW + (k >> 1) * 64 * WROW, v[k]);
    }
    DRAIN();
    BAR();
    f32x4 Wr[4], Wi[4];
    {
        LAS char* rp = lds + B_WIMG + (4 * g + (c >> 2)) * WROW + 32 * w + 8 * (c & 3);
        const unsigned sg = (8 * G.oct + 2 * w >= 12) ? 0x80000000u : 0u;
#pragma unroll
        for (int t = 0; t < 4; ++t) {
            const u32x2 a = trread(rp + 16 * t * WROW), bq = trread(rp + (64 + 16 * t) * WROW);
            Wr[t] = (f32x4){bflo(a[0]), bfhi(a[0]), bflo(a[1]), bfhi(a[1])};
            Wi[t] = __builtin_bit_cast(f32x4, (u32x4){(bq[0] << 16) ^ sg, (bq[0] & 0xffff0000u) ^ sg, (bq[1] << 16) ^ sg, (bq[1] & 0xffff0000u) ^ sg});
            PIN(Wr[t]); PIN(Wi[t]);
        }
    }
    u32x4 Pf[4], Qf[4], Pi[4], Qi[4];
    {
        LAS char* tp = lds + B_P2T + l * 16;
#pragma unroll
        for (int t = 0; t < 4; ++t) { Pf[t] = ldsr<u32x4>(tp + t * 1024); Qf[t] = ldsr<u32x4>(tp + (4 + t) * 1024); Pi[t] = ldsr<u32x4>(tp + (8 + t) * 1024); Qi[t] = ldsr<u32x4>(tp + (12 + t) * 1024); }
#pragma unroll
        for (int t = 0; t < 4; ++t) { PIN(Pf[t]); PIN(Qf[t]); PIN(Pi[t]); PIN(Qi[t]); }
    }
    unsigned lo = E1OFF + (unsigned)(row * 64 + 16 * g) * ROWB + (unsigned)p * 4;
    unsigned so = E2OFF + (unsigned)(4 * c) * SLABG + e2_rowoff(row) + (unsigned)(pos0 + 4 * g) * 4;
    PIN(lo); PIN(so);
    STAMP(5);
    wait_flags(X, F_E1 + (unsigned)(row * 64 + l) * 64);
    STAMP(6);
    u32x4 Xv[4];
#pragma unroll 1
    for (unsigned sp = 0; sp < SPIN_CAP; ++sp) {
#pragma unroll
        for (int t = 0; t < 4; ++t)
#pragma unroll
            for (int i = 0; i < 4; ++i) Xv[t][i] = __builtin_amdgcn_raw_buffer_load_b32(X.rs, lo, (t + 4 * i) * ROWB, SC1);
        if (!HINT_E1) break;
        if (__all(max(max(max4(Xv[0]), max4(Xv[1])), max(max4(Xv[2]), max4(Xv[3]))) != ~0u)) break;
        asm volatile("" ::: "memory");
    }
    STAMP(7);
    f32x4 R[4], I[4];
#pragma unroll
    for (int t = 0; t < 4; ++t) { R[t] = mfma16(Pf[t], Xv[t], (f32x4){}); I[t] = mfma16(Qf[t], Xv[t], (f32x4){}); }
    radix4(R, I);
#pragma unroll
    for (int t = 0; t < 4; ++t) { const f32x4 zr = R[t] * Wr[t] - I[t] * Wi[t], zi = -(R[t] * Wi[t]) - I[t] * Wr[t]; R[t] = zr; I[t] = zi; }
    radix4(R, I);
#pragma unroll
    for (int t = 0; t < 4; ++t) {
        const u32x4 hq = {pk(R[t][0], I[t][0]), pk(R[t][1], I[t][1]), pk(R[t][2], I[t][2]), pk(R[t][3], I[t][3])};
        const f32x4 oR = mfma16(hq, Pi[t], (f32x4){}), oI = mfma16(hq, Qi[t], (f32x4){});
        const u32x4 v = {pk(oR[0], oI[0]), pk(oR[1], oI[1]), pk(oR[2], oI[2]), pk(oR[3], oI[3])};
        __builtin_amdgcn_raw_buffer_store_b128(v, X.rs, so, t * SLABG, SC1);
    }
    STAMP(8);
}

DI void phase_p3(const Params& P, const Ctx& X, bool owner, int idx, int s2, int j, int w, int l, LAS char* lds STAMP_ARG) {
    const int c = l & 15, g = l >> 4;
    {
        const int pr = c >> 2, o = c & 3, sa = 64 * (8 * j + 2 * pr) + s2, m = 4 * g + w;
        const float aa = (float)((sa * m) & 2047) * (1.0f / 2048.0f), ab = (float)(((sa + 64) * m) & 2047) * (1.0f / 2048.0f);
        const float sc = 1.0f / (2048.0f * 576.0f);
        const float ca = cosr(aa) * sc, sna = sinr(aa) * sc, cb = cosr(ab) * sc, snb = sinr(ab) * sc;
        const float al = ca - snb, be = sna + cb, ga = ca + snb, de = cb - sna;
        const unsigned v0 = pk(al, -be), v1 = pk(be, al), v2 = pk(ga, de), v3 = pk(de, -ga);
        const unsigned lo = (o & 1) ? v1 : v0, hi = (o & 1) ? v3 : v2;
        const unsigned s16 = pk(sc, sc) ^ ((o & 1) ? 0x8000u : 0u) ^ ((o & 2) ? 0x80000000u : 0u);
        ldsw<unsigned>(lds + T_M3 + l * 16 + w * 4, (o & 2) ? hi : lo);
        ldsw<unsigned>(lds + T_M3 + 1024 + l * 16 + w * 4, m == 0 ? s16 : (o & 2) ? lo : hi);
    }
    if (owner) { DRAIN(); BAR(); if (threadIdx.x == 0) publish(X, F_E2 + (unsigned)idx * 64); }
    LAS char *bD, *b0, *bM, *bM0, *b16[3], *bzm, *bzp, *aD, *aM, *a16, *a0[2], *azm, *azp;
    {
        const int c7 = c & 7, c3 = c >> 3, low = (8 - c7) & 7;
        LAS char* sl = lds + B_SLAB; LAS char* rb = sl + 768 + 9280 * g; LAS char* zi = lds + B_ZI + g * IMGB;
        const bool g0 = g == 0;
        const int i = w >> 1, m0 = 3 * (w & 1);
        const bool z = i == 0 && c7 == 0;
        const int pd = 192 * i + 16 * m0 + c, pm = (z ? 8 : 192 * ((c7 ? 2 : 3) - i) + low) + 152 - 8 * c3 - 16 * m0;
        bD = rb + 4 * pd; bM = rb + 4 * pm; bM0 = (w == 0 && c == 0) ? rb : bM + 128;
        b0 = g0 ? sl + 4 * pd + 768 * i : bD;
        b16[0] = g0 ? b0 + 768 : bM0; b16[1] = g0 ? b0 + 832 : bM + 64; b16[2] = g0 ? b0 + 896 : bM;
        bzm = zi + 4 * (224 * i + 28 * c7 + c3 + 2 * m0);
        bzp = zi + 4 * ((z ? 1 : 672 - 224 * i - 28 * c7) + 19 - c3 - 2 * m0);
        if (w < 3) {
            const int pm1 = (c7 ? low : 192) + 168 - 8 * c3 - 32 * w;
            aD = rb + 4 * (384 + 32 * w + c); aM = rb + 4 * pm1;
            a16 = g0 ? sl + 4 * (pm1 + (c7 ? 192 : 384)) : aM;
            a0[0] = g0 ? a16 - 704 : aD; a0[1] = g0 ? a16 - 768 : aD + 64;
            azm = zi + 4 * (448 + 28 * c7 + c3 + 4 * w); azp = zi + 4 * (224 - 28 * c7 + 21 - c3 - 4 * w);
        } else {
            aD = rb + 384; aM = rb + 320; a16 = g0 ? sl + 1088 : aM; a0[0] = g0 ? sl + 384 : aD; a0[1] = a0[0];
            azm = zi + 96; azp = zi + 40;
        }
        PIN(bD); PIN(b0); PIN(bM); PIN(bM0); PIN(b16[0]); PIN(b16[1]); PIN(b16[2]); PIN(bzm); PIN(bzp);
        PIN(aD); PIN(aM); PIN(a16); PIN(a0[0]); PIN(a0[1]); PIN(azm); PIN(azp);
    }
    unsigned cj = (g == 0 && w < 3) ? 0x80000000u : 0u;
    PIN(cj);
    DftC C;
    {
        LAS char* t = lds + B_TAB + l * 16;
#pragma unroll
        for (int s = 0; s < 3; ++s) { C.P1[s] = ldsr<u32x4>(t + s * 1024); C.Q1[s] = ldsr<u32x4>(t + (3 + s) * 1024); C.tc[s] = ldsr<f32x4>(t + (6 + s) * 1024); C.ts[s] = ldsr<f32x4>(t + (9 + s) * 1024); }
#pragma unroll
        for (int s = 0; s < 3; ++s) { PIN(C.P1[s]); PIN(C.Q1[s]); PIN(C.tc[s]); PIN(C.ts[s]); C.P2[s] = C.P1[s]; C.Q2[s] = C.Q1[s]; }
    }
    const int a = l & 31, h = l >> 5;
    LAS char* zr = lds + B_ZI + w * IMGB + ((a < 24 ? a : 23) * 28 + 4 * h) * 4;
    unsigned oo = (unsigned)(64 * (8 * j + 2 * w) + s2) * 2304 + 1152 + (24 * a + 4 * h) * 2, l16 = l * 16;
    PIN(zr); PIN(oo); PIN(l16);
    const unsigned sb = E2OFF + (unsigned)s2 * SLABG + w * 9216;
    const u32x4 srd = slot_srd(X.slot);
    STAMP(10);
    if (w == 0) wait_flags3(X, F_E2 + (unsigned)l * 64, F_E2 + (unsigned)(64 + l) * 64, F_E2 + (unsigned)(l < 19 ? 128 + l : 0) * 64);
    BAR();
    const u32x4 M3a = ldsr<u32x4>(lds + T_M3 + l * 16), M3b = ldsr<u32x4>(lds + T_M3 + 1024 + l * 16);
    STAMP(11);
    u32x4 z[3];
#pragma unroll 1
    for (unsigned sp = 0; sp < SPIN_CAP; ++sp) {
#pragma unroll
        for (int k = 0; k < 3; ++k) dma16x3_sc1(srd, l16, sb + k * 3072, B_SLAB + w * 9216 + k * 3072);
        if (w == 3) dma16_sc1(srd, l16, sb + 9216, B_SLAB + 36864);
        asm volatile("s_waitcnt vmcnt(0)\n\ts_barrier" ::: "memory");
        STAMP(12);
        u32x4 f0[5], f1[5];
#pragma unroll
        for (int k = 0; k < 5; ++k) {
            LAS char* pD = k < 3 ? bD + 64 * k : aD + 64 * (k - 3);
            LAS char* pM = k == 0 ? bM0 : k < 3 ? bM + 64 * (2 - k) : aM + 64 * (4 - k);
            f0[k][0] = ldsr<unsigned>(k < 3 ? b0 + 64 * k : a0[k - 3]); f1[k][0] = ldsr<unsigned>(k < 3 ? b16[k] : a16 + 64 * (4 - k));
#pragma unroll
            for (int i = 1; i < 4; ++i) { f0[k][i] = ldsr<unsigned>(pD + i * 2304); f1[k][i] = ldsr<unsigned>(pM + i * 2304); }
        }
        unsigned mx = 0;
        f32x4 d[5];
#pragma unroll
        for (int k = 0; k < 5; ++k) {
            if (HINT_E2) mx = max(mx, max(max4(f0[k]), max4(f1[k])));
            if (k >= 3) { f0[k][0] ^= cj; f1[k][0] ^= cj; }
            d[k] = mfma16(M3b, f1[k], mfma16(M3a, f0[k], (f32x4){}));
        }
#pragma unroll
        for (int k = 0; k < 5; ++k) if (k < 4 || w < 3) {
            ldsw<unsigned>(k < 3 ? bzm + 8 * k : azm + 8 * (k - 3), pk(d[k][2], d[k][3]));
            ldsw<unsigned>(k < 3 ? bzp + 8 * (2 - k) : azp + 8 * (4 - k), pk(d[k][0], d[k][1]));
        }
        if (HINT_E2) ldsw<unsigned>(lds + L_FAIL + 4 * w, __any(mx == ~0u) ? 1u : 0u);
        BAR();
        STAMP(13);
#pragma unroll
        for (int s = 0; s < 3; ++s) z[s] = ldsr<u32x4>(zr + 32 * s);
        if (!HINT_E2) break;
        const u32x4 f = ldsr<u32x4>(lds + L_FAIL);
        if (rfl(f[0] | f[1] | f[2] | f[3]) == 0) break;
        BAR();
    }
    f32x16 oR, oI;
    dft576(z, C, oR, oI);
    const rsrc_t ro = __builtin_amdgcn_make_buffer_rsrc((void*)P.out, 0, 2048 * 2304, 0x00020000);
    if (a < 24) {
#pragma unroll
        for (int i = 0; i < 3; ++i) {
            __builtin_amdgcn_raw_buffer_store_b64((u32x2){pk(oR[4 * i], oR[4 * i + 1]), pk(oR[4 * i + 2], oR[4 * i + 3])}, ro, oo + 16 * i, 0, 0);
            __builtin_amdgcn_raw_buffer_store_b64((u32x2){pk(oI[4 * i], oI[4 * i + 1]), pk(oI[4 * i + 2], oI[4 * i + 3])}, ro, oo + 16 * i, 64 * 2304, 0);
        }
    }
    STAMP(14);
}

DI int rowperm(int c) { return (c & 8) + 2 * (c & 3) + ((c >> 2) & 1); }
DI void phase_t1(const Ctx& X, int cg, int lb, int w, int l, LAS char* lds, const u32x4 (&ur)[3] STAMP_ARG) {
    const int c = l & 15, g = l >> 4, q = c >> 2, pp = c & 3;
    LAS char* wp = lds + A_STG + (16 * w + rowperm(c)) * 176 + 16 * g;
    LAS char* rp = lds + A_STG + (32 * (w & 1) + 8 * g + q) * 176 + 32 * (w >> 1) + 8 * pp;
    unsigned so = UTOFF + (unsigned)(72 * cg + 16 * (w >> 1) + c) * 4096 + (unsigned)(64 * lb + 32 * (w & 1) + 8 * g) * 2;
    PIN(wp); PIN(rp); PIN(so);
    ldsw<u32x4>(wp, ur[0]); ldsw<u32x4>(wp + 64, ur[1]); if (g == 0) ldsw<u32x4>(wp + 128, ur[2]);
    BAR();
#pragma unroll
    for (int it = 0; it < 3; ++it) {
        if (it < 2 || w < 2) {
            const u32x2 lo = trread(rp + 64 * it), hi = trread(rp + 64 * it + 4 * 176);
            if (it < 2 || c < 8) __builtin_amdgcn_raw_buffer_store_b128((u32x4){lo[0], lo[1], hi[0], hi[1]}, X.rs, so, it * 32 * 4096, SC1);
        }
    }
}
DI f32x2 zpow(float al2, float brev, float p) { const float e = __builtin_amdgcn_exp2f(al2 * p), t = __builtin_amdgcn_fractf(brev * p); return (f32x2){e * cosr(t), e * sinr(t)}; }
struct Modes { unsigned la, aim, cr, ci, ldt; };
DI Modes load_modes(const Params& P, int ch, int half, int l) {
    const int idx = ch * 128 + half * 64 + l;
    return {((const GAS u16*)P.logA)[idx], ((const GAS u16*)P.Aim)[idx], ((const GAS u16*)P.Cre)[idx], ((const GAS u16*)P.Cim)[idx], ((const GAS u16*)P.logdt)[ch]};
}
DI void kgen_tables(const Modes& M, LAS char* tab, int l) {
    const float dt = bfround(expf(bflo(M.ldt)));
    const float al2 = -bfround(expf(bflo(M.la))) * dt * 1.44269504088896f, brev = bflo(M.aim) * dt * 0.15915494309189535f;
    const f32x2 z8 = zpow(al2, brev, 8.f), z32 = zpow(al2, brev, 32.f), y1 = zpow(al2, -brev, 1.f), y256 = zpow(al2, -brev, 256.f);
    LAS char* Lp = tab + l * 4; LAS char* Hp = tab + 32 * 272 + l * 4;
    f32x2 L[4]; L[0] = (f32x2){1.f, 0.f}; L[1] = z8; L[2] = cmul(z8, z8); L[3] = cmul(L[2], z8);
    f32x2 H[8]; H[0] = (f32x2){bflo(M.cr), -bflo(M.ci)} * (2.f * dt);
#pragma unroll
    for (int jj = 0; jj < 8; ++jj) {
#pragma unroll
        for (int k = 0; k < 4; ++k) { ldsw<unsigned>(Lp + (4 * jj + k) * 272, pk2(L[k])); if (jj < 7) L[k] = cmul(L[k], z32); }
        ldsw<unsigned>(Hp + jj * 272, pk2(H[jj])); if (jj < 7) H[jj + 1] = cmul(H[jj], y1);
    }
#pragma unroll
    for (int k = 0; k < 4; ++k) { H[2 * k] = cmul(H[2 * k], y256); ldsw<unsigned>(Hp + (8 + k) * 272, pk2(H[2 * k])); }
#pragma unroll
    for (int k = 0; k < 4; ++k) { const int s = 4 * (k & 1); H[s] = cmul(H[s], y256); ldsw<unsigned>(Hp + (12 + k) * 272, pk2(H[s])); }
#pragma unroll
    for (int k = 0; k < 5; ++k) { H[0] = cmul(H[0], y256); ldsw<unsigned>(Hp + (16 + k) * 272, pk2(H[0])); }
}
DI void kgen_contract(LAS char* tab, LAS char* kp, int l) {
    const int jc = l & 31, h = l >> 5;
    LAS char* ap = tab + (32 + (jc < 21 ? jc : 20)) * 272 + 16 * h; LAS char* bp = tab + jc * 272 + 16 * h;
    u32x4 A[8], B[8];
#pragma unroll
    for (int s = 0; s < 8; ++s) { A[s] = ldsr<u32x4>(ap + 32 * s); B[s] = ldsr<u32x4>(bp + 32 * s); }
    f32x16 d = {};
#pragma unroll
    for (int s = 0; s < 8; ++s) d = mfma32(A[s], B[s], d);
    ldsw<f32x4>(kp + (8 * jc + 4 * h) * 4, (f32x4){d[0], d[1], d[2], d[3]});
    ldsw<f32x2>(kp + (h ? 384 + 2 * jc : 256 + 4 * jc) * 4, (f32x2){d[4], d[5]});
    ldsw<f32x2>(kp + (h ? 448 + 2 * jc : 258 + 4 * jc) * 4, (f32x2){d[6], d[7]});
#pragma unroll
    for (int i = 0; i < 4; ++i) ldsw<float>(kp + (512 + 128 * h + 32 * i + jc) * 4, d[8 + i]);
}
DI void kexpand(LAS char* k0, LAS char* dst, int w, int tid) {
    LAS char* k1 = k0 + 3072;
    u32x4 v; unsigned prev;
    if (w == 0) {
        const bool s0 = tid < 32;
        const int base = (s0 ? 8 * tid : 128 + 4 * tid) * 4, pb = tid ? base - 4 : 0;
        const f32x4 a = ldsr<f32x4>(k0 + base) + ldsr<f32x4>(k1 + base), b = ldsr<f32x4>(k0 + base + 16) + ldsr<f32x4>(k1 + base + 16);
        const float p = ldsr<float>(k0 + pb) + ldsr<float>(k1 + pb);
        v = (u32x4){pk(s0 ? b[3] : b[0], s0 ? b[2] : a[3]), pk(s0 ? b[1] : a[2], s0 ? b[0] : a[2]), pk(s0 ? a[3] : a[2], s0 ? a[2] : a[1]), pk(s0 ? a[1] : a[0], a[0])};
        prev = pk(tid == 0 ? 0.f : tid <= 32 ? p : a[0], 0.f);
    } else if (w == 1) {
        const int base = (256 + 2 * tid) * 4;
        const f32x2 a = ldsr<f32x2>(k0 + base) + ldsr<f32x2>(k1 + base); const float b = ldsr<float>(k0 + base + 8) + ldsr<float>(k1 + base + 8);
        prev = pk(a[0], a[0]);
        v = (u32x4){pk(b, b), pk(a[1], a[1]), pk(a[1], a[0]), prev};
    } else {
        const int base = (384 + tid) * 4;
        const float v0 = ldsr<float>(k0 + base) + ldsr<float>(k1 + base), v1 = ldsr<float>(k0 + base + 4) + ldsr<float>(k1 + base + 4);
        prev = pk(v0, v0);
        v = (u32x4){pk(v1, v1), pk(v1, (tid & 1) ? v1 : v0), prev, prev};
    }
    ldsw<u32x4>(dst + C_K1, v);
    ldsw<u32x4>(dst + C_K0, (u32x4){__builtin_amdgcn_alignbit(v[1], v[0], 16), __builtin_amdgcn_alignbit(v[2], v[1], 16), __builtin_amdgcn_alignbit(v[3], v[2], 16), __builtin_amdgcn_alignbit(prev, v[3], 16)});
}
struct Frags { u32x4 F[8], G[8]; };
DI void frag_load(Frags& S, int i, LAS char* fp, LAS char* gp) {
    const u32x2 a = *(volatile LAS u32x2*)(fp + 512 * i), b = *(volatile LAS u32x2*)(fp + 512 * i + 8);
    S.F[i] = (u32x4){a[0], a[1], b[0], b[1]};
    S.G[i] = *(volatile LAS u32x4*)(gp + 512 * (7 - i));
}
DI void conv_step(f32x4 (&acc)[8], const Frags& C, Frags& N, LAS char* fp, LAS char* gp) {
#pragma unroll
    for (int t = 0; t < 8; ++t) {
        frag_load(N, t, fp, gp);
#pragma unroll
        for (int i = 0; i <= t; ++i) acc[t] = mfma16(C.F[i], C.G[t - i], acc[t]);
    }
#pragma unroll
    for (int k = 0; k < 24; ++k) { __builtin_amdgcn_sched_group_barrier(0x008, 1, 0); __builtin_amdgcn_sched_group_barrier(0x100, 1, 0); }
    __builtin_amdgcn_sched_group_barrier(0x008, 12, 0);
}
DI void store_y(const Ctx& X, unsigned off, int t, f32x4 v) {
    __builtin_amdgcn_raw_buffer_store_b64((u32x2){pk(v[0], v[1]), pk(v[2], v[3])}, X.rs, off, 512 * t, SC1);
}
DI void conv_job(const Ctx& X, const Modes& M, int ch0, int nch, int cg, unsigned myflag, unsigned utflag, int w, int l, LAS char* lds STAMP_ARG) {
    const int tid = threadIdx.x;
    const bool two = nch == 2;
    LAS char* kp = lds + 2 * nch * TABB;
    if (two || w < 2) kgen_tables(M, lds + w * TABB, l);
    STAMP(5);
    if (two) { DRAIN(); BAR(); if (tid == 0) publish(X, utflag); }
    if (!two) { DRAIN(); if (l == 0) publish(X, utflag); }
    if (two || w < 2) kgen_contract(lds + w * TABB, kp + w * 3072, l);
    const int kch = two ? (w >> 1) : 0, res0 = two ? 4 * (w & 1) : 2 * w;
    const int r = l & 15, g = l >> 4;
    LAS char* fp = lds + kch * CHB + ((r & 1) ? 52 : C_U2 + 48) + 4 * r - 16 * g + 64 * res0;
    LAS char* gp = lds + kch * CHB + ((r & 1) ? C_K1 : C_K0) + 496 - 64 * (r >> 1) - 16 * g + 64 * res0;
    unsigned yo = YTOFF + (unsigned)(ch0 + kch) * 4096 + (unsigned)(64 * (r >> 1) + 16 * g + 8 * (r & 1)), l16 = l * 16;
    LAS char* dp = lds + nch * CHB + l * 16;
    LAS char* zp = lds + kch * CHB + (l < 4 ? ((w & 1) ? C_U2 : 0) : ((w & 1) ? C_K1 : C_K0) + 4032) + 16 * l;
    LAS char* kd = lds + (2040 - 8 * tid) * 2;
    PIN(fp); PIN(gp); PIN(yo); PIN(dp); PIN(zp); PIN(kd); PIN(l16);
    const u32x4 srd = slot_srd(X.slot);
    STAMP(6);
    BAR();
    if (two || w < 2) {
        if (l < 33) ldsw<u32x4>(zp, (u32x4){0, 0, 0, 0});
        wait_flags(X, F_UT + (unsigned)(cg * 32 + (l & 31)) * 64);
        asm volatile("s_waitcnt lgkmcnt(0)" ::: "memory");
        dma16x4_sc1(srd, l16, UTOFF + (unsigned)(ch0 + kch) * 4096, kch * CHB + ((w & 1) ? C_U2 + 60 : 64));
    }
    STAMP(7);
    kexpand(kp, kd, w, tid);
    if (two) kexpand(kp + 6144, kd + CHB, w, tid);
    STAMP(8);
    asm volatile("s_waitcnt vmcnt(0) lgkmcnt(0)\n\ts_barrier" ::: "memory");
    STAMP(9);
    f32x4 acc[8];
#pragma unroll
    for (int t = 0; t < 8; ++t) acc[t] = (f32x4){};
    Frags S0, S1;
#pragma unroll
    for (int i = 0; i < 8; ++i) frag_load(S0, i, fp, gp);
#pragma unroll 1
    for (int it = 0; it < nch; ++it) {
        conv_step(acc, S0, S1, fp + 64, gp + 64);
        conv_step(acc, S1, S0, fp + 128, gp + 128);
        fp += 128; gp += 128;
    }
    STAMP(10);
    if (two) {
        if (w & 1) {
#pragma unroll
            for (int t = 0; t < 4; ++t) ldsw<f32x4>(dp + (w * 4 + t) * 1024, acc[t]);
        } else {
#pragma unroll
            for (int t = 0; t < 4; ++t) ldsw<f32x4>(dp + (w * 4 + t) * 1024, acc[4 + t]);
        }
        BAR();
        LAS char* op = dp + (w ^ 1) * 4096;
        if (w & 1) {
#pragma unroll
            for (int t = 0; t < 4; ++t) store_y(X, yo, 4 + t, acc[4 + t] + ldsr<f32x4>(op + t * 1024));
        } else {
#pragma unroll
            for (int t = 0; t < 4; ++t) store_y(X, yo, t, acc[t] + ldsr<f32x4>(op + t * 1024));
        }
    } else {
#pragma unroll
        for (int t = 0; t < 8; ++t) ldsw<f32x4>(dp + (w * 8 + t) * 1024, acc[t]);
        BAR();
        LAS char* op = dp + w * 2048;
#pragma unroll
        for (int k = 0; k < 2; ++k)
            store_y(X, yo + 1024 * w, k, (ldsr<f32x4>(op + k * 1024) + ldsr<f32x4>(op + (8 + k) * 1024)) + (ldsr<f32x4>(op + (16 + k) * 1024) + ldsr<f32x4>(op + (24 + k) * 1024)));
    }
    STAMP(11);
    DRAIN(); BAR();
    if (tid == 0) publish(X, myflag);
}
DI void phase_t2(const Params& P, const Ctx& X, int cg, int lb, int w, int l, LAS char* lds, u32x4 (&ur)[3], const u32x4 (&dv)[3] STAMP_ARG) {
    const int tid = threadIdx.x, c = l & 15, g = l >> 4, q = c >> 2, pp = c & 3;
#pragma unroll
    for (int m = 0; m < 3; ++m) {
#pragma unroll
        for (int i = 0; i < 4; ++i) ur[m][i] = pk(bflo(ur[m][i]) * bflo(dv[m][i]), bfhi(ur[m][i]) * bfhi(dv[m][i]));
        PIN(ur[m]);
    }
    unsigned go = YTOFF + (unsigned)(72 * cg + (tid >> 3)) * 4096 + (unsigned)(128 * lb + 16 * (tid & 7));
    unsigned go2 = YTOFF + (unsigned)(72 * cg + 64 + (l >> 3)) * 4096 + (unsigned)(128 * lb + 16 * (tid & 7));
    LAS char* wp = lds + (tid >> 3) * 144 + 16 * (tid & 7);
    LAS char* rp = lds + (8 * g + q) * 144 + 32 * w + 8 * pp;
    LAS char* rp2 = lds + (64 + q) * 144 + 32 * w + 8 * pp;
    unsigned oo = (unsigned)(64 * lb + 16 * w + rowperm(c)) * 2304 + cg * 144 + 16 * g;
    PIN(go); PIN(go2); PIN(wp); PIN(rp); PIN(rp2); PIN(oo);
    wait_flags(X, F_YT + (unsigned)(l < 32 ? cg * 32 + l : 256 + cg * 8 + (l & 7)) * 64);
    STAMP(12);
    u32x4 yv[3];
#pragma unroll 1
    for (unsigned sp = 0; sp < SPIN_CAP; ++sp) {
        yv[0] = __builtin_amdgcn_raw_buffer_load_b128(X.rs, go, 0, SC1); yv[1] = __builtin_amdgcn_raw_buffer_load_b128(X.rs, go, 32 * 4096, SC1);
        yv[2] = yv[0];
        if (w == 0) yv[2] = __builtin_amdgcn_raw_buffer_load_b128(X.rs, go2, 0, SC1);
        if (!HINT_YT) break;
        if (__all(max(max(max4(yv[0]), max4(yv[1])), max4(yv[2])) != ~0u)) break;
        asm volatile("" ::: "memory");
    }
    ldsw<u32x4>(wp, yv[0]); ldsw<u32x4>(wp + 32 * 144, yv[1]); if (w == 0) ldsw<u32x4>(wp + 64 * 144, yv[2]);
    BAR();
    STAMP(13);
    const rsrc_t ro = __builtin_amdgcn_make_buffer_rsrc((void*)P.out, 0, 2048 * 2304, 0x00020000);
#pragma unroll
    for (int m = 0; m < 3; ++m) {
        LAS char* p = m < 2 ? rp + 32 * 144 * m : rp2;
        const u32x2 lo = trread(p), hi = trread(p + 4 * 144);
        const u32x4 y = {lo[0], lo[1], hi[0], hi[1]};
        u32x4 r;
#pragma unroll
        for (int i = 0; i < 4; ++i) r[i] = pk(bflo(y[i]) + bflo(ur[m][i]), bfhi(y[i]) + bfhi(ur[m][i]));
        if (m < 2 || g == 0) __builtin_amdgcn_raw_buffer_store_b128(r, ro, oo + 64 * m, 0, 0);
    }
    STAMP(14);
}

__global__ void __launch_bounds__(256, 2) fused_kernel(Params P) {
    __shared__ __attribute__((aligned(16))) char smem[LDS_BYTES];
    LAS char* lds = (LAS char*)smem;
    const int b = blockIdx.x, tid = threadIdx.x, l = tid & 63, w = rfl(tid >> 6);
    STAMP(0);
    const u32x4 tsrd = make_srd(&TABLES, TABLE_BYTES);
    if (b < 32) {
        const int v = (b >> 3) * 4 + w;
        if (v < 4) touch(make_srd((const void*)(__builtin_amdgcn_s_getpc() & ~127ull), CODE_PF), l * 128, v * 8192, L_JUNK);
        else touch(tsrd, l * 128, (v - 4) * 8192, L_JUNK);
    }
    if (b < 256 && w == 3) {
        const GAS char* a = l < 12 ? (const GAS char*)P.ws + ((size_t)l << 20) : l < 17 ? (const GAS char*)P.out + ((size_t)(l - 12) << 20) : l < 19 ? (const GAS char*)P.wr + ((size_t)(l - 17) << 20) : l < 21 ? (const GAS char*)P.wi + ((size_t)(l - 19) << 20) : (const GAS char*)P.ws;
        asm volatile("s_mov_b32 m0, %1\n\ts_nop 0\n\tglobal_load_lds_dword %0, off" :: "v"(a), "s"(L_JUNK + 512) : "memory");
    }
    const int epo = (b < 256 ? L_JUNK + 256 : 81408) + 16 * w;
    epoch_issue(P, l, epo);
    LAS char* ep = lds + epo;
    const bool roleB = b < 256;
    const int s2 = (b & 7) * 8 + ((b >> 3) & 7), j = (b >> 6) & 3, cg = b & 7, lb = (b >> 3) & 31;
    const bool extra = b < 64;
    P2Geom G;
    const int ch0 = roleB ? 72 * cg + 64 + ((b >> 3) & 7) : 72 * cg + 2 * lb;
    u32x4 ur[3], dv[3];
    Modes M = {};
    Ctx X;
    if (roleB) {
        PRIO(3);
        p1_issue(P, tsrd, s2, j, w, l);
        asm volatile("" ::: "memory");
        if (extra) M = load_modes(P, ch0, w & 1, l);
        asm volatile("" ::: "memory");
        phase_p1(P, X, ep, s2, j, w, l, lds STAMP_PASS);
        int bb = b; asm volatile("" : "+s"(bb));
        G = p2_geom(bb);
        const unsigned myflag = F_E1 + (unsigned)((4 * j + w) * 64 + s2) * 64;
        BAR();
        if (G.owner) phase_p2(P, X, G, tsrd, myflag, w, l, lds STAMP_PASS);
        else {
            if (!extra) { DRAIN(); if (l == 0) publish(X, myflag); }
            if (b == 255 && w == 0) {
#pragma unroll 1
                for (int k = 0; k < 4; ++k) { wait_flags(X, F_UT + (unsigned)(64 * k + l) * 64); wait_flags(X, F_E1 + (unsigned)(256 * k + l) * 64); }
                if (l == 0) __hip_atomic_store((GAS u64*)(P.ws + EPOCH_OFF), X.tag, RLX_AGENT);
            }
        }
    } else {
        PRIO(1);
        const int c = l & 15, g = l >> 4;
        const GAS char* src = (const GAS char*)P.u + (size_t)(64 * lb + 16 * w + rowperm(c)) * 1152 + cg * 144;
        ur[0] = *(const GAS u32x4*)(src + 16 * g); ur[1] = *(const GAS u32x4*)(src + 64 + 16 * g); ur[2] = *(const GAS u32x4*)(src + 128);
        M = load_modes(P, ch0 + (w >> 1), w & 1, l);
        const GAS char* dsrc = (const GAS char*)P.Dskip + cg * 144;
        dv[0] = *(const GAS u32x4*)(dsrc + 16 * g); dv[1] = *(const GAS u32x4*)(dsrc + 64 + 16 * g); dv[2] = *(const GAS u32x4*)(dsrc + 128);
        asm volatile("" : "+v"(ur[0]) :: "memory");
        X = make_ctx(P, ep);
        STAMP(1);
        phase_t1(X, cg, lb, w, l, lds, ur STAMP_PASS);
        STAMP(2);
    }
    if (!roleB || extra) {
        conv_job(X, M, ch0, roleB ? 1 : 2, cg, F_YT + (unsigned)(roleB ? 256 + cg * 8 + ((b >> 3) & 7) : cg * 32 + lb) * 64,
                 roleB ? F_E1 + (unsigned)((4 * j + w) * 64 + s2) * 64 : F_UT + (unsigned)(cg * 32 + lb) * 64, w, l, lds STAMP_PASS);
    }
    if (roleB) phase_p3(P, X, G.owner, G.idx, s2, j, w, l, lds STAMP_PASS);
    else phase_t2(P, X, cg, lb, w, l, lds, ur, dv STAMP_PASS);
}

extern "C" void kernel_launch(void* const* d_in, const int* in_sizes, int n_in, void* d_out, int out_size, void* d_ws, size_t ws_size, hipStream_t stream) {
    Params P;
    P.u = (const u16*)d_in[0]; P.logA = (const u16*)d_in[1]; P.Aim = (const u16*)d_in[2]; P.Cre = (const u16*)d_in[3]; P.Cim = (const u16*)d_in[4];
    P.logdt = (const u16*)d_in[5]; P.Dskip = (const u16*)d_in[6]; P.x = (const u16*)d_in[7]; P.wr = (const u16*)d_in[8]; P.wi = (const u16*)d_in[9];
    P.out = (u16*)d_out; P.ws = (char*)d_ws;
    fused_kernel<<<512, 256, 0, stream>>>(P);
}
```

```cpp
#include <hip/hip_runtime.h>
#include <hip/hip_bf16.h>
#include <stdint.h>

#define DL_E1 10
#define DL_E2 12
#define DL_UT 14
#define DL_UTX 5
#define DL_YT 20
#define DL_YTX 28
#define DL_UTN 19
#define OAUX 16
#define TMID 2
#define GAS __attribute__((address_space(1)))
#define LAS __attribute__((address_space(3)))
#define DI __device__ __forceinline__
typedef __bf16 bf16x8 __attribute__((ext_vector_type(8)));
typedef __bf16 bf16x2 __attribute__((ext_vector_type(2)));
typedef float f32x2 __attribute__((ext_vector_type(2)));
typedef float f32x4 __attribute__((ext_vector_type(4)));
typedef float f32x16 __attribute__((ext_vector_type(16)));
typedef unsigned u32x2 __attribute__((ext_vector_type(2)));
typedef unsigned u32x4 __attribute__((ext_vector_type(4)));
typedef short s16x4 __attribute__((ext_vector_type(4)));
typedef unsigned short u16;
typedef unsigned long long u64;
typedef __amdgpu_buffer_rsrc_t rsrc_t;
#define RLX_AGENT __ATOMIC_RELAXED, __HIP_MEMORY_SCOPE_AGENT
#define RARE(x) __builtin_expect(!!(x), 0)
#define PIN(x) asm volatile("" : "+v"(x))
#define BAR() asm volatile("s_waitcnt lgkmcnt(0)\n\ts_barrier" ::: "memory")
#define DRAIN() asm volatile("s_waitcnt vmcnt(0)" ::: "memory")
constexpr int SC1 = 16;

DI unsigned pk(float a, float b) { f32x2 v = {a, b}; return __builtin_bit_cast(unsigned, __builtin_convertvector(v, bf16x2)); }
DI unsigned pk2(f32x2 v) { return __builtin_bit_cast(unsigned, __builtin_convertvector(v, bf16x2)); }
DI float bflo(unsigned v) { return __builtin_bit_cast(float, v << 16); }
DI float bfhi(unsigned v) { return __builtin_bit_cast(float, v & 0xffff0000u); }
DI float bfround(float x) { return bflo(pk(x, x)); }
DI f32x4 mfma16(u32x4 a, u32x4 b, f32x4 c) { return __builtin_amdgcn_mfma_f32_16x16x32_bf16(__builtin_bit_cast(bf16x8, a), __builtin_bit_cast(bf16x8, b), c, 0, 0, 0); }
DI f32x16 mfma32(u32x4 a, u32x4 b, f32x16 c) { return __builtin_amdgcn_mfma_f32_32x32x16_bf16(__builtin_bit_cast(bf16x8, a), __builtin_bit_cast(bf16x8, b), c, 0, 0, 0); }
DI float cosr(float x) { return __builtin_amdgcn_cosf(x); }
DI float sinr(float x) { return __builtin_amdgcn_sinf(x); }
DI u32x2 trread(LAS char* p) { return __builtin_bit_cast(u32x2, __builtin_amdgcn_ds_read_tr16_b64_v4i16((LAS s16x4*)p)); }
template <class T> DI T ldsr(LAS char* p) { return *(LAS T*)p; }
DI unsigned ldsv(LAS char* p) { return *(volatile LAS unsigned*)p; }
template <class T> DI void ldsw(LAS char* p, T v) { *(LAS T*)p = v; }
DI float opq(float x) { asm("" : "+v"(x)); return x; }
DI f32x4 add4(f32x4 a, f32x4 b) { return (f32x4){opq(a[0] + b[0]), opq(a[1] + b[1]), opq(a[2] + b[2]), opq(a[3] + b[3])}; }
DI int rfl(int x) { return __builtin_amdgcn_readfirstlane(x); }
DI f32x2 cmul(f32x2 a, f32x2 b) { const f32x2 t = a * b.xx; return __builtin_elementwise_fma(a.yx, (f32x2){-b.y, b.y}, t); }

struct Params {
    const u16 *u, *logA, *Aim, *Cre, *Cim, *logdt, *Dskip, *x, *wr, *wi;
    u16* out; char* ws;
};

constexpr unsigned ROWB = 3072;
constexpr unsigned TROW = 4352;
constexpr unsigned FSTR = 32;
constexpr unsigned EOFF = 0, TOFF = 1024 * ROWB, F_E1 = TOFF + 576 * TROW, F_E2 = F_E1 + 1024 * FSTR, F_UT = F_E2 + 8 * 192 * FSTR, F_YT = F_UT + 8 * 192 * FSTR;
constexpr unsigned EPOCH_OFF = F_YT + 8 * 320 * FSTR + 256, WS_USED = EPOCH_OFF + 256;
constexpr int B_XM = 0, B_XT = 32768, B_SLAB = 0, B_P2T = 0, B_WIMG = 36864, WROW = 112, B_PAR = 51200, L_JUNK = 55296;
constexpr int B_ZI = 55744, IMGB = 2704, B_TAB = 66560, T_M1 = B_TAB + 12288, T_M3 = B_TAB + 13312;
constexpr int C_U2 = 4224, C_K0 = 8448, C_K1 = C_K0 + 4576, CHB = C_K1 + 4576, TABB = 53 * 272, A_JUNK = 61440, A_STG = 65536, A_PAR = 77824;
constexpr int LDS_BYTES = 81920;
constexpr unsigned SPIN_CAP = 1u << 18;

#define VMWAIT(n) asm volatile("s_waitcnt vmcnt(" #n ")" ::: "memory")
#define PRIO(n) __builtin_amdgcn_s_setprio(n)

DI u32x4 make_srd(const void* p, unsigned bytes) { const u64 a = (u64)p; return (u32x4){(unsigned)a, (unsigned)(a >> 32) & 0xffffu, bytes, 0x00020000u}; }
DI void dma16(u32x4 srd, unsigned voff, unsigned soff, unsigned dst) {
    asm volatile("s_mov_b32 m0, %3\n\ts_nop 0\n\tbuffer_load_dwordx4 %0, %1, %2 offen lds" :: "v"(voff), "s"(srd), "s"(soff), "s"(dst) : "memory");
}
DI void dma16x4_sc1(u32x4 srd, unsigned voff, unsigned soff, unsigned dst) {
    asm volatile("s_mov_b32 m0, %3\n\ts_nop 0\n\tbuffer_load_dwordx4 %0, %1, %2 offen sc1 lds\n\tbuffer_load_dwordx4 %0, %1, %2 offen offset:1024 sc1 lds\n\t"
                 "buffer_load_dwordx4 %0, %1, %2 offen offset:2048 sc1 lds\n\tbuffer_load_dwordx4 %0, %1, %2 offen offset:3072 sc1 lds" :: "v"(voff), "s"(srd), "s"(soff), "s"(dst) : "memory");
}
DI void dma16_sc1(u32x4 srd, unsigned voff, unsigned soff, unsigned dst) {
    asm volatile("s_mov_b32 m0, %3\n\ts_nop 0\n\tbuffer_load_dwordx4 %0, %1, %2 offen sc1 lds" :: "v"(voff), "s"(srd), "s"(soff), "s"(dst) : "memory");
}
DI void touch(u32x4 srd, unsigned voff, unsigned soff, unsigned dst) {
    asm volatile("s_nop 4\n\ts_mov_b32 m0, %3\n\ts_nop 0\n\tbuffer_load_dword %0, %1, %2 offen lds" :: "v"(voff), "s"(srd), "s"(soff), "s"(dst) : "memory");
}

struct Ctx { GAS char* ws; u64 tag; };
DI rsrc_t ws_rsrc(const Ctx& X) { return __builtin_amdgcn_make_buffer_rsrc((void*)X.ws, 0, WS_USED, 0x00020000); }
DI u32x4 ws_srd(const Ctx& X) { return make_srd((const void*)X.ws, WS_USED); }
DI void publish(const Ctx& X, unsigned off) { __hip_atomic_store((GAS u64*)(X.ws + off), X.tag, RLX_AGENT); }
DI void publish8(const Ctx& X, unsigned off, unsigned stride) { if (threadIdx.x < 8) publish(X, off + threadIdx.x * stride); }
DI u64 peek(const Ctx& X, unsigned off) { return __hip_atomic_load((GAS u64*)(X.ws + off), RLX_AGENT); }
DI void wait_flags(const Ctx& X, unsigned off) {
#pragma unroll 1
    for (unsigned sp = 0; sp < SPIN_CAP; ++sp) {
        if (__all(peek(X, off) == X.tag)) break;
        __builtin_amdgcn_s_sleep(1);
    }
    __builtin_amdgcn_fence(__ATOMIC_ACQUIRE, "wavefront");
}
DI void wait_flags2(const Ctx& X, unsigned o0, unsigned o1) {
#pragma unroll 1
    for (unsigned sp = 0; sp < SPIN_CAP; ++sp) {
        const u64 f0 = peek(X, o0), f1 = peek(X, o1);
        if (__all(f0 == X.tag && f1 == X.tag)) break;
        __builtin_amdgcn_s_sleep(1);
    }
    __builtin_amdgcn_fence(__ATOMIC_ACQUIRE, "wavefront");
}
DI void wait_flags3(const Ctx& X, unsigned o0, unsigned o1, unsigned o2) {
#pragma unroll 1
    for (unsigned sp = 0; sp < SPIN_CAP; ++sp) {
        const u64 f0 = peek(X, o0), f1 = peek(X, o1), f2 = peek(X, o2);
        if (__all(f0 == X.tag && f1 == X.tag && f2 == X.tag)) break;
        __builtin_amdgcn_s_sleep(1);
    }
    __builtin_amdgcn_fence(__ATOMIC_ACQUIRE, "wavefront");
}
DI void modes_issue(const Params& P, int ch, int half, int l, unsigned dst) {
    const unsigned vo = ch * 256 + half * 128 + l * 4, vd = (ch * 2) & ~3;
    if (l < 32) asm volatile("s_mov_b32 m0, %7\n\ts_nop 0\n\tglobal_load_lds_dword %0, %2\n\ts_add_u32 m0, m0, 128\n\ts_nop 0\n\tglobal_load_lds_dword %0, %3\n\t"
                             "s_add_u32 m0, m0, 128\n\ts_nop 0\n\tglobal_load_lds_dword %0, %4\n\ts_add_u32 m0, m0, 128\n\ts_nop 0\n\tglobal_load_lds_dword %0, %5\n\t"
                             "s_add_u32 m0, m0, 128\n\ts_nop 0\n\tglobal_load_lds_dword %1, %6"
                             :: "v"(vo), "v"(vd), "s"(P.logA), "s"(P.Aim), "s"(P.Cre), "s"(P.Cim), "s"(P.logdt), "s"(dst) : "memory", "scc");
}
DI void misc_issue(const Params& P, int ch, int l, unsigned dst) {
    const unsigned ve = EPOCH_OFF + (l & 1) * 4, vd = ch * 2 + l * 4;
    if (l < 48) asm volatile("s_mov_b32 m0, %4\n\ts_nop 0\n\tglobal_load_lds_dword %0, %2 sc1\n\ts_add_u32 m0, m0, 192\n\ts_nop 0\n\tglobal_load_lds_dword %1, %3"
                             :: "v"(ve), "v"(vd), "s"(P.ws), "s"(P.Dskip), "s"(dst + 640) : "memory", "scc");
}
DI u64 read_tag(LAS char* par) {
    const u32x2 e = ldsr<u32x2>(par + 640);
    return (((u64)(unsigned)rfl(e[1]) << 32) | (unsigned)rfl(e[0])) + 0x9E3779B97F4A7C15ull;
}

struct DftC { u32x4 P1[3], Q1[3], P2[3], Q2[3]; f32x4 tc[3], ts[3]; };
DI void dft576(const u32x4 (&z)[3], const DftC& C, f32x16& oR, f32x16& oI) {
    f32x16 aR = {}, aI = {};
#pragma unroll
    for (int s = 0; s < 3; ++s) { aR = mfma32(z[s], C.P1[s], aR); aI = mfma32(z[s], C.Q1[s], aI); }
    oR = (f32x16){}; oI = (f32x16){};
#pragma unroll
    for (int s = 0; s < 3; ++s) {
        u32x4 y;
#pragma unroll
        for (int k = 0; k < 2; ++k) {
            const f32x2 r = {aR[4 * s + 2 * k], aR[4 * s + 2 * k + 1]}, i = {aI[4 * s + 2 * k], aI[4 * s + 2 * k + 1]};
            const f32x2 c = {C.tc[s][2 * k], C.tc[s][2 * k + 1]}, sn = {C.ts[s][2 * k], C.ts[s][2 * k + 1]};
            const f32x2 yr = __builtin_elementwise_fma(i, sn, r * c), yi = __builtin_elementwise_fma(-r, sn, i * c);
            y[2 * k] = pk(yr[0], yi[0]); y[2 * k + 1] = pk(yr[1], yi[1]);
        }
        oR = mfma32(y, C.P2[s], oR); oI = mfma32(y, C.Q2[s], oI);
    }
}

struct P2Geom { int row, mp; };
DI P2Geom p2_geom(int b) {
    const int k = b >> 3;
    return {k < 12 ? (b & 7) + 8 : (b & 7), k < 12 ? k : k - 12};
}
DI int w_col0(const P2Geom& G) { return G.mp >= 6 ? 529 - 48 * G.mp : 48 * G.mp; }
DI void w_issue(const Params& P, int kap, const P2Geom& G, int w, int l, unsigned dst) {
    const bool mir = G.mp >= 6;
    const unsigned n = 128 * w + l, k2 = (n * 9363u) >> 16, n2 = n + 64, k3 = (n2 * 9363u) >> 16;
    const int base = ((mir ? 2048 - kap : kap) * 578 + 2 * w_col0(G)) & ~3, step = mir ? -18496 - 112 : 18496 - 112;
    unsigned v0 = base + step * (int)k2 + 16 * n, v1 = base + step * (int)k3 + 16 * n2;
    if (RARE(mir && kap == 0 && w == 0)) v0 = k2 == 0 ? v0 - 2048 * 578 : v0;
    const u32x4 sr = make_srd(P.wr, 2048 * 578), si = make_srd(P.wi, 2048 * 578);
    asm volatile("s_mov_b32 m0, %3\n\ts_nop 0\n\tbuffer_load_dwordx4 %0, %1, 0 offen lds\n\ts_add_u32 m0, m0, 0x1c00\n\ts_nop 0\n\tbuffer_load_dwordx4 %0, %2, 0 offen lds"
                 :: "v"(v0), "s"(sr), "s"(si), "s"(dst + 2048 * w) : "memory", "scc");
    if (w < 3) asm volatile("s_mov_b32 m0, %3\n\ts_nop 0\n\tbuffer_load_dwordx4 %0, %1, 0 offen lds\n\ts_add_u32 m0, m0, 0x1c00\n\ts_nop 0\n\tbuffer_load_dwordx4 %0, %2, 0 offen lds"
                            :: "v"(v1), "s"(sr), "s"(si), "s"(dst + 2048 * w + 1024) : "memory", "scc");
}
DI void wedge_issue(const Params& P, int kap, int mp, int l, unsigned dst) {
    const int ke = kap + 32 * l;
    const unsigned e = (mp ? ke * 289 + 288 : ((2048 - ke) & 2047) * 289) * 2;
    asm volatile("s_mov_b32 m0, %3\n\ts_nop 0\n\tglobal_load_lds_ushort %0, %1\n\ts_add_u32 m0, m0, 0x100\n\ts_nop 0\n\tglobal_load_lds_ushort %0, %2"
                 :: "v"(e), "s"(P.wr), "s"(P.wi), "s"(dst) : "memory", "scc");
}

DI void p1_issue(const Params& P, int s2, int w, int l) {
    const u32x4 xs = make_srd(P.x, 2048 * 1152);
    const unsigned so = (unsigned)(512 * w + s2) * 1152, l16 = l * 16, dst = B_XM + 8192 * w;
#pragma unroll
    for (int i = 0; i < 8; ++i) {
        unsigned sc, vc;
        asm volatile("s_add_u32 m0, %5, %7\n\ts_add_u32 %0, %4, %6\n\tv_xor_b32 %1, %8, %2\n\tbuffer_load_dwordx4 %1, %3, %0 offen lds"
                     : "=&s"(sc), "=&v"(vc) : "v"(l16), "s"(xs), "s"(so), "s"(dst), "i"(i * 64 * 1152), "i"(i * 1024), "i"(32 * i) : "memory", "scc");
    }
    const int rr = l >> 3;
    dma16(xs, rr * (64 * 1152) + 1024 + (((l & 7) ^ (rr & 6)) * 16), so, B_XT + 1024 * w);
}
DI void p1_tables(int w, int l, LAS char* lds) {
    const int n = l & 31, h = l >> 5;
    LAS char* tp = lds + l * 16 + w * 4;
#pragma unroll
    for (int s = 0; s < 3; ++s) {
        const float f = (float)((8 * s + 4 * h + w) * n), a1 = f * (1.0f / 24.0f), a2 = f * (1.0f / 576.0f);
        const float c1 = cosr(a1), s1 = sinr(a1);
        const unsigned p1 = pk(c1, s1);
        ldsw<unsigned>(tp + B_TAB + s * 1024, p1); ldsw<unsigned>(tp + B_TAB + (3 + s) * 1024, __builtin_amdgcn_alignbit(p1, p1, 16) ^ 0x8000u);
        ldsw<float>(tp + B_TAB + (6 + s) * 1024, cosr(a2)); ldsw<float>(tp + B_TAB + (9 + s) * 1024, sinr(a2));
    }
}
DI void phase_p1(Ctx& X, LAS char* par, int s2, int j, int w, int l, LAS char* lds) {
    const int c = l & 15, g = l >> 4, q = c >> 2, pp = c & 3, r = 4 * g + q;
    {
        const int i = c & 3, kap = 4 * j + (c >> 2), s1 = (w < 2 ? 0 : 12) + 4 * g + 2 * w;
        const float qq = i == 1 ? 0.25f : 0.f;
        float a0 = (float)(((64 * s1 + s2) * kap) & 2047) * (1.0f / 2048.0f) + qq, a1 = (float)(((64 * s1 + 64 + s2) * kap) & 2047) * (1.0f / 2048.0f) + qq;
        if (kap == 0 && i == 1) { a0 = 0.f; a1 = 0.5f; }
        ldsw<unsigned>(lds + T_M1 + l * 16 + w * 4, i < 2 ? pk(cosr(a0), cosr(a1)) : 0u);
    }
    LAS char* x0 = lds + B_XM + r * 1024 + (2 * (w ^ q) + (pp >> 1)) * 16 + 8 * (pp & 1);
    LAS char* xe = x0 + 128 * (g & 1); LAS char* xo = x0 - 128 * (g & 1);
    LAS char* xt = lds + B_XT + r * 128 + ((2 * w + (pp >> 1)) ^ (r & 6)) * 16 + 8 * (pp & 1);
    LAS char* za[3];
#pragma unroll
    for (int m = 0; m < 3; ++m) { const unsigned d = 64 * m + 16 * w + c, a = (d * 2731u) >> 16; za[m] = lds + B_ZI + g * IMGB + (112 * d - 2684 * a); PIN(za[m]); }
    const bool special = (j == 0 && w == 0);
    const int kb = l & 31, h = l >> 5;
    LAS char* zr = lds + B_ZI + w * IMGB + ((kb < 24 ? kb : 23) * 28 + 4 * h) * 4;
    LAS char* t2 = lds + B_TAB + l * 16; LAS char* t1 = special ? lds + B_TAB + ((l & 39) + ((l >> 1) & 8)) * 16 : t2;
    unsigned so = EOFF + (unsigned)((4 * j + w) * 64 + s2) * ROWB + (unsigned)(kb * 8 + h * 4) * 4;
    PIN(xe); PIN(xo); PIN(xt); PIN(zr); PIN(so); PIN(t1); PIN(t2);
    p1_tables(w, l, lds);
    BAR();
    DftC C;
#pragma unroll
    for (int s = 0; s < 3; ++s) {
        C.P1[s] = ldsr<u32x4>(t1 + s * 1024); C.Q1[s] = ldsr<u32x4>(t1 + (3 + s) * 1024); C.tc[s] = ldsr<f32x4>(t1 + (6 + s) * 1024); C.ts[s] = ldsr<f32x4>(t1 + (9 + s) * 1024);
        C.P2[s] = ldsr<u32x4>(t2 + s * 1024); C.Q2[s] = ldsr<u32x4>(t2 + (3 + s) * 1024);
    }
    u32x4 M1 = ldsr<u32x4>(lds + T_M1 + l * 16);
    if (RARE(special)) {
        const unsigned sp = (l & 8) ? 0x03020c0cu : 0x0c0c0302u, sq = (l & 8) ? 0x01000c0cu : 0x0c0c0100u;
#pragma unroll
        for (int s = 0; s < 3; ++s)
#pragma unroll
            for (int i = 0; i < 4; ++i) { const unsigned qv = C.Q1[s][i]; C.P1[s][i] = __builtin_amdgcn_perm(qv, qv, sp); C.Q1[s][i] = __builtin_amdgcn_perm(qv, qv, sq); }
        const float ah = (l & 8) ? (float)s2 * (1.0f / 128.0f) : 0.f, ch = cosr(ah), sh = sinr(ah);
#pragma unroll
        for (int s = 0; s < 3; ++s) { const f32x4 tc = C.tc[s], ts = C.ts[s]; C.tc[s] = tc * ch - ts * sh; C.ts[s] = ts * ch + tc * sh; }
    }
#pragma unroll
    for (int s = 0; s < 3; ++s) { PIN(C.P1[s]); PIN(C.Q1[s]); PIN(C.tc[s]); PIN(C.ts[s]); PIN(C.P2[s]); PIN(C.Q2[s]); }
    PIN(M1);
    if (j != 3) DRAIN(); else VMWAIT(7);
    BAR();
    {
        u32x2 lo[9], hi[9];
#pragma unroll
        for (int m = 0; m < 9; ++m) { LAS char* p = m == 8 ? xt : ((m & 1) ? xo : xe) + 128 * m; lo[m] = trread(p); hi[m] = trread(p + (m == 8 ? 2048 : 16384)); }
        f32x4 d[9];
#pragma unroll
        for (int m = 0; m < 9; ++m) d[m] = mfma16(M1, (u32x4){lo[m][0], lo[m][1], hi[m][0], hi[m][1]}, (f32x4){});
#pragma unroll
        for (int m = 0; m < 9; ++m) ldsw<unsigned>(za[m % 3] + 32 * (m / 3), pk(d[m][0], d[m][1]));
        __builtin_amdgcn_sched_group_barrier(0x100, 18, 0);
#pragma unroll
        for (int m = 0; m < 3; ++m) __builtin_amdgcn_sched_group_barrier(0x008, 1, 0);
#pragma unroll
        for (int m = 0; m < 6; ++m) { __builtin_amdgcn_sched_group_barrier(0x008, 1, 0); __builtin_amdgcn_sched_group_barrier(0x002, 1, 0); __builtin_amdgcn_sched_group_barrier(0x200, 1, 0); }
    }
    if (j == 3) DRAIN();
    X.tag = read_tag(par);
    BAR();
    u32x4 z[3];
#pragma unroll
    for (int s = 0; s < 3; ++s) z[s] = ldsr<u32x4>(zr + 32 * s);
    f32x16 oR, oI;
    dft576(z, C, oR, oI);
    const rsrc_t rs = ws_rsrc(X);
    if (kb < 24) {
#pragma unroll
        for (int i = 0; i < 3; ++i) {
            const u32x4 v = {pk(oR[4 * i], oI[4 * i]), pk(oR[4 * i + 1], oI[4 * i + 1]), pk(oR[4 * i + 2], oI[4 * i + 2]), pk(oR[4 * i + 3], oI[4 * i + 3])};
            __builtin_amdgcn_raw_buffer_store_b128(v, rs, so + 768 * i, 0, SC1);
        }
        if (RARE(special)) __builtin_amdgcn_raw_buffer_store_b128((u32x4){pk(oR[12], oI[12]), pk(oR[13], oI[13]), pk(oR[14], oI[14]), pk(oR[15], oI[15])}, rs, so + 2304, 0, SC1);
    }
}

DI void radix4_out(float (&R)[4][4], float (&I)[4][4], int i, float aR, float aI, float bR, float bI, float cR, float cI, float dR, float dI) {
    R[0][i] = opq(aR + cR); I[0][i] = opq(aI + cI); R[2][i] = opq(aR - cR); I[2][i] = opq(aI - cI);
    R[1][i] = opq(bR + dI); I[1][i] = opq(bI - dR); R[3][i] = opq(bR - dI); I[3][i] = opq(bI + dR);
}
DI void phase_p2(const Params& P, const Ctx& X, const P2Geom& G, unsigned myflag, int w, int l, LAS char* lds) {
    const int c = l & 15, g = l >> 4, row = G.row;
    const bool spec = row == 0, active = w < 3 || spec, mir = G.mp >= 6;
    const int kap = spec ? 16 * (w & 1) : row, ko = spec ? 8 * (w >> 1) : 8 * w;
    const bool edge = ko == 0 && (G.mp == 0 || G.mp == 6);
    const int img0 = spec ? 38912 : B_WIMG, edg = spec ? 53248 + 512 * w : B_PAR;
    DRAIN(); if (l == 0) publish(X, myflag);
    w_issue(P, row, G, w, l, img0);
    if (RARE(spec)) w_issue(P, 16, G, w, l, 24576);
    if (RARE(edge)) wedge_issue(P, kap, G.mp, l, edg);
    u32x4 Pf[4], Qf[4], Pi[4], Qi[4];
    PRIO(0);
    {
        const int uu = 4 * g + w;
        LAS char* tp = lds + B_P2T + l * 16 + w * 4;
        const float a0 = (float)(c * uu) * (1.0f / 16.0f), ac = (float)c * (1.0f / 64.0f), au = (float)uu * (1.0f / 64.0f);
        float c1 = cosr(a0), s1 = sinr(a0), c2 = c1, s2 = s1;
        const float cc = cosr(ac), sc = sinr(ac), cu = cosr(au), su = sinr(au);
#pragma unroll
        for (int t = 0; t < 4; ++t) {
            const unsigned p1 = pk(c1, s1), p2 = pk(c2, s2);
            ldsw<unsigned>(tp + t * 1024, p1); ldsw<unsigned>(tp + (4 + t) * 1024, __builtin_amdgcn_alignbit(p1, p1, 16) ^ 0x8000u);
            ldsw<unsigned>(tp + (8 + t) * 1024, p2); ldsw<unsigned>(tp + (12 + t) * 1024, __builtin_amdgcn_alignbit(p2, p2, 16) ^ 0x80000000u);
            if (t < 3) {
                const float n1 = opq(__builtin_fmaf(c1, cc, -opq(s1 * sc))), m1 = opq(__builtin_fmaf(s1, cc, opq(c1 * sc)));
                const float n2 = opq(__builtin_fmaf(c2, cu, -opq(s2 * su))), m2 = opq(__builtin_fmaf(s2, cu, opq(c2 * su)));
                c1 = n1; s1 = m1; c2 = n2; s2 = m2;
            }
        }
        if (RARE(spec)) {
#pragma unroll
            for (int t = 0; t < 4; ++t) {
                const float a3 = (float)((2 * uu + 1) * (t + 4 * c)) * (1.0f / 128.0f), c3 = cosr(a3), s3 = sinr(a3);
                ldsw<unsigned>(tp + (16 + t) * 1024, pk(c3, s3)); ldsw<unsigned>(tp + (20 + t) * 1024, pk(s3, -c3));
            }
        }
    }
    const int rel = 24 * (c >> 3) + ko + (c & 7);
    const int par = (kap + w_col0(G)) & 1;
    LAS char* wp = lds + (kap == 16 ? 24576 : img0) + 4 * g * WROW + 2 * (par + (mir ? 47 - rel : rel));
    unsigned lo = EOFF + (unsigned)(row * 64 + 16 * g) * ROWB + (unsigned)(192 * w + 16 * G.mp + c) * 4;
    unsigned so = EOFF + (unsigned)(row * 64 + 4 * c) * ROWB + (unsigned)(192 * w + 16 * G.mp + 4 * g) * 4;
    PIN(lo); PIN(so); PIN(wp);
    PRIO(3);
    if (w == 3) { __builtin_amdgcn_s_sleep(DL_E1); wait_flags(X, F_E1 + (unsigned)(row * 64 + l) * FSTR); }
    BAR();
    if (!active) { BAR(); return; }
    const rsrc_t rs = ws_rsrc(X);
    u32x4 Xv[4];
#pragma unroll
    for (int t = 0; t < 4; ++t)
#pragma unroll
        for (int i = 0; i < 4; ++i) Xv[t][i] = __builtin_amdgcn_raw_buffer_load_b32(rs, lo, (t + 4 * i) * ROWB, SC1);
    __builtin_amdgcn_sched_barrier(0);
    f32x4 Wr[4], Wi[4];
    {
        VMWAIT(16); BAR();
        if (RARE(edge)) {
            LAS char* pw = lds + (kap == 16 ? 24576 : img0) + l * WROW + 2 * (par + (mir ? 47 : 0));
            const unsigned e = pk(opq(0.5f * opq(bflo(ldsr<u16>(pw)) + bflo(ldsr<unsigned>(lds + edg + 4 * l)))), opq(0.5f * opq(bflo(ldsr<u16>(pw + 7168)) - bflo(ldsr<unsigned>(lds + edg + 256 + 4 * l)))));
            ldsw<u16>(pw, (u16)e); ldsw<u16>(pw + 7168, (u16)(e >> 16));
        }
        LAS char* tp = lds + B_P2T + l * 16; LAS char* ti = tp + (kap == 16 ? 16384 : 8192);
#pragma unroll
        for (int t = 0; t < 4; ++t) { Pf[t] = ldsr<u32x4>(tp + t * 1024); Qf[t] = ldsr<u32x4>(tp + (4 + t) * 1024); Pi[t] = ldsr<u32x4>(ti + t * 1024); Qi[t] = ldsr<u32x4>(ti + (4 + t) * 1024); }
        const unsigned sg = mir ? 0x80000000u : 0u;
#pragma unroll
        for (int t = 0; t < 4; ++t)
#pragma unroll
            for (int i = 0; i < 4; ++i) {
                Wr[t][i] = bflo(ldsr<u16>(wp + (16 * t + i) * WROW));
                Wi[t][i] = __builtin_bit_cast(float, ((unsigned)ldsr<u16>(wp + 7168 + (16 * t + i) * WROW) << 16) ^ sg);
            }
#pragma unroll
        for (int t = 0; t < 4; ++t) { PIN(Pf[t]); PIN(Qf[t]); PIN(Pi[t]); PIN(Qi[t]); PIN(Wr[t]); PIN(Wi[t]); }
    }
    float R[4][4], I[4][4];
    {
        const f32x4 r0 = mfma16(Pf[0], Xv[0], (f32x4){}), m0 = mfma16(Qf[0], Xv[0], (f32x4){}), r1 = mfma16(Pf[1], Xv[1], (f32x4){}), m1 = mfma16(Qf[1], Xv[1], (f32x4){});
        const f32x4 ra = mfma16(Pf[2], Xv[2], r0), ma = mfma16(Qf[2], Xv[2], m0), rc = mfma16(Pf[3], Xv[3], r1), mc = mfma16(Qf[3], Xv[3], m1);
#pragma unroll
        for (int i = 0; i < 4; ++i)
            radix4_out(R, I, i, ra[i], ma[i], opq(__builtin_fmaf(2.f, r0[i], -ra[i])), opq(__builtin_fmaf(2.f, m0[i], -ma[i])),
                       rc[i], mc[i], opq(__builtin_fmaf(2.f, r1[i], -rc[i])), opq(__builtin_fmaf(2.f, m1[i], -mc[i])));
#pragma unroll
        for (int i = 0; i < 4; ++i) {
            float s[2][4];
#pragma unroll
            for (int t = 0; t < 2; ++t) {
                const float zr = opq(__builtin_fmaf(-I[t][i], Wi[t][i], opq(R[t][i] * Wr[t][i]))), zi = opq(__builtin_fmaf(-I[t][i], Wr[t][i], -opq(R[t][i] * Wi[t][i])));
                s[t][0] = opq(__builtin_fmaf(-I[t + 2][i], Wi[t + 2][i], opq(__builtin_fmaf(R[t + 2][i], Wr[t + 2][i], zr))));
                s[t][1] = opq(__builtin_fmaf(-I[t + 2][i], Wr[t + 2][i], opq(__builtin_fmaf(-R[t + 2][i], Wi[t + 2][i], zi))));
                s[t][2] = opq(__builtin_fmaf(2.f, zr, -s[t][0])); s[t][3] = opq(__builtin_fmaf(2.f, zi, -s[t][1]));
            }
            radix4_out(R, I, i, s[0][0], s[0][1], s[0][2], s[0][3], s[1][0], s[1][1], s[1][2], s[1][3]);
        }
    }
#pragma unroll
    for (int t = 0; t < 4; ++t) {
        const u32x4 hq = {pk(R[t][0], I[t][0]), pk(R[t][1], I[t][1]), pk(R[t][2], I[t][2]), pk(R[t][3], I[t][3])};
        const f32x4 oR = mfma16(hq, Pi[t], (f32x4){}), oI = mfma16(hq, Qi[t], (f32x4){});
        const u32x4 v = {pk(oR[0], oI[0]), pk(oR[1], oI[1]), pk(oR[2], oI[2]), pk(oR[3], oI[3])};
        __builtin_amdgcn_raw_buffer_store_b128(v, rs, so, t * ROWB, SC1);
    }
}

DI void phase_p3(const Params& P, const Ctx& X, unsigned pubflag, unsigned pubstride, int s2, int j, int w, int l, LAS char* lds) {
    const int c = l & 15, g = l >> 4;
    PRIO(0);
    {
        const int pr = c >> 2, o = c & 3, sa = 64 * (8 * j + 2 * pr) + s2, m = 4 * g + w;
        const float aa = opq((float)((sa * m) & 2047) * (1.0f / 2048.0f)), ab = opq((float)(((sa + 64) * m) & 2047) * (1.0f / 2048.0f));
        const float sc = 1.0f / (2048.0f * 576.0f);
        const float ca = opq(cosr(aa) * sc), sna = opq(sinr(aa) * sc), cb = opq(cosr(ab) * sc), snb = opq(sinr(ab) * sc);
        const float al = opq(ca - snb), be = opq(sna + cb), ga = opq(ca + snb), de = opq(cb - sna);
        const unsigned v0 = pk(al, -be), v1 = pk(be, al), v2 = pk(ga, de), v3 = pk(de, -ga);
        const unsigned lo = (o & 1) ? v1 : v0, hi = (o & 1) ? v3 : v2;
        const unsigned s16 = pk(sc, sc) ^ ((o & 1) ? 0x8000u : 0u) ^ ((o & 2) ? 0x80000000u : 0u);
        ldsw<unsigned>(lds + T_M3 + l * 16 + w * 4, (o & 2) ? hi : lo);
        ldsw<unsigned>(lds + T_M3 + 1024 + l * 16 + w * 4, m == 0 ? s16 : (o & 2) ? lo : hi);
    }
    DRAIN(); BAR(); publish8(X, pubflag, pubstride);
    LAS char *bD, *b0, *bM, *bM0, *b16[3], *bzm, *bzp, *aD, *aM, *a16, *a0[2], *azm, *azp;
    {
        const int c7 = c & 7, c3 = c >> 3, low = (8 - c7) & 7;
        LAS char* sl = lds + B_SLAB; LAS char* rb = sl + 768 + 9280 * g; LAS char* zi = lds + B_ZI + g * IMGB;
        const bool g0 = g == 0;
        const int i = w >> 1, m0 = 3 * (w & 1);
        const bool z = i == 0 && c7 == 0;
        const int pd = 192 * i + 16 * m0 + c, pm = (z ? 8 : 192 * ((c7 ? 2 : 3) - i) + low) + 152 - 8 * c3 - 16 * m0;
        bD = rb + 4 * pd; bM = rb + 4 * pm; bM0 = (w == 0 && c == 0) ? rb : bM + 128;
        b0 = g0 ? sl + 4 * pd + 768 * i : bD;
        b16[0] = g0 ? b0 + 768 : bM0; b16[1] = g0 ? b0 + 832 : bM + 64; b16[2] = g0 ? b0 + 896 : bM;
        bzm = zi + 4 * (224 * i + 28 * c7 + c3 + 2 * m0);
        bzp = zi + 4 * ((z ? 1 : 672 - 224 * i - 28 * c7) + 19 - c3 - 2 * m0);
        if (w < 3) {
            const int pm1 = (c7 ? low : 192) + 168 - 8 * c3 - 32 * w;
            aD = rb + 4 * (384 + 32 * w + c); aM = rb + 4 * pm1;
            a16 = g0 ? sl + 4 * (pm1 + (c7 ? 192 : 384)) : aM;
            a0[0] = g0 ? a16 - 704 : aD; a0[1] = g0 ? a16 - 768 : aD + 64;
            azm = zi + 4 * (448 + 28 * c7 + c3 + 4 * w); azp = zi + 4 * (224 - 28 * c7 + 21 - c3 - 4 * w);
        } else {
            aD = rb + 384; aM = rb + 320; a16 = g0 ? sl + 1088 : aM; a0[0] = g0 ? sl + 384 : aD; a0[1] = a0[0];
            azm = zi + 96; azp = zi + 40;
        }
        PIN(bD); PIN(b0); PIN(bM); PIN(bM0); PIN(b16[0]); PIN(b16[1]); PIN(b16[2]); PIN(bzm); PIN(bzp);
        PIN(aD); PIN(aM); PIN(a16); PIN(a0[0]); PIN(a0[1]); PIN(azm); PIN(azp);
    }
    unsigned cj = (g == 0 && w < 3) ? 0x80000000u : 0u;
    PIN(cj);
    DftC C;
    {
        LAS char* t = lds + B_TAB + l * 16;
#pragma unroll
        for (int s = 0; s < 3; ++s) { C.P1[s] = ldsr<u32x4>(t + s * 1024); C.Q1[s] = ldsr<u32x4>(t + (3 + s) * 1024); C.tc[s] = ldsr<f32x4>(t + (6 + s) * 1024); C.ts[s] = ldsr<f32x4>(t + (9 + s) * 1024); }
#pragma unroll
        for (int s = 0; s < 3; ++s) { PIN(C.P1[s]); PIN(C.Q1[s]); PIN(C.tc[s]); PIN(C.ts[s]); C.P2[s] = C.P1[s]; C.Q2[s] = C.Q1[s]; }
    }
    const int a = l & 31, h = l >> 5;
    LAS char* zr = lds + B_ZI + w * IMGB + ((a < 24 ? a : 23) * 28 + 4 * h) * 4;
    LAS char* ow = lds + B_ZI + w * IMGB + 48 * a + 8 * h;
    LAS char* orr = lds + B_ZI + w * IMGB + l * 16; LAS char* ot = lds + B_ZI + w * IMGB + 1024 + (l & 7) * 16 + ((l >> 3) & 1) * 1152;
    unsigned oo = (unsigned)(64 * (8 * j + 2 * w) + s2) * 2304 + 1152 + l * 16, l16 = l * 16;
    unsigned oot = oo + 1024 - (l & ~7) * 16 + (l >> 3) * (64 * 2304);
    PIN(zr); PIN(ow); PIN(orr); PIN(ot); PIN(oo); PIN(oot); PIN(l16);
    const u32x4 srd = ws_srd(X);
    constexpr unsigned RS = 64 * ROWB - 2304;
    const unsigned r0x = w == 0 ? 768 : 0, ab = EOFF + (unsigned)(256 * w + s2) * ROWB, ad = B_SLAB + 768 + 9280 * w;
    unsigned v2 = l16 + (l < 16 ? r0x : RS), v4 = l16 + (l < 32 ? 0 : RS), v6 = l16 + (l < 48 ? 0 : RS);
    PIN(v2); PIN(v4); PIN(v6);
    PRIO(3);
    if (w == 0) { __builtin_amdgcn_s_sleep(DL_E2); const unsigned f = F_E2 + (unsigned)((s2 >> 3) * 192 + l) * FSTR; wait_flags3(X, f, f + 64 * FSTR, f + 128 * FSTR); }
    BAR();
    const u32x4 M3a = ldsr<u32x4>(lds + T_M3 + l * 16), M3b = ldsr<u32x4>(lds + T_M3 + 1024 + l * 16);
    u32x4 z[3];
    {
        dma16_sc1(srd, l16, ab + r0x, ad); dma16_sc1(srd, l16, ab + r0x + 1024, ad + 1024); dma16_sc1(srd, v2, ab + 2048, ad + 2048);
        dma16_sc1(srd, l16, ab + RS + 3072, ad + 3072); dma16_sc1(srd, v4, ab + RS + 4096, ad + 4096); dma16_sc1(srd, l16, ab + 2 * RS + 5120, ad + 5120);
        dma16_sc1(srd, v6, ab + 2 * RS + 6144, ad + 6144); dma16_sc1(srd, l16, ab + 3 * RS + 7168, ad + 7168); dma16_sc1(srd, l16, ab + 3 * RS + 8192, ad + 8192);
        if (w == 3 && l < 48) dma16_sc1(srd, l16, EOFF + (unsigned)s2 * ROWB, B_SLAB);
        asm volatile("s_waitcnt vmcnt(0)\n\ts_barrier" ::: "memory");
        u32x4 f0[5], f1[5];
#pragma unroll
        for (int k = 0; k < 5; ++k) {
            LAS char* pD = k < 3 ? bD + 64 * k : aD + 64 * (k - 3);
            LAS char* pM = k == 0 ? bM0 : k < 3 ? bM + 64 * (2 - k) : aM + 64 * (4 - k);
            f0[k][0] = ldsv(k < 3 ? b0 + 64 * k : a0[k - 3]); f1[k][0] = ldsv(k < 3 ? b16[k] : a16 + 64 * (4 - k));
#pragma unroll
            for (int i = 1; i < 4; ++i) { f0[k][i] = ldsv(pD + i * 2304); f1[k][i] = ldsv(pM + i * 2304); }
        }
        f32x4 d[5];
#pragma unroll
        for (int k = 0; k < 5; ++k) {
            if (k >= 3) { f0[k][0] ^= cj; f1[k][0] ^= cj; }
            d[k] = mfma16(M3b, f1[k], mfma16(M3a, f0[k], (f32x4){}));
        }
#pragma unroll
        for (int k = 0; k < 5; ++k) if (k < 4 || w < 3) {
            ldsw<unsigned>(k < 3 ? bzm + 8 * k : azm + 8 * (k - 3), pk(d[k][2], d[k][3]));
            ldsw<unsigned>(k < 3 ? bzp + 8 * (2 - k) : azp + 8 * (4 - k), pk(d[k][0], d[k][1]));
        }
        BAR();
#pragma unroll
        for (int s = 0; s < 3; ++s) z[s] = ldsr<u32x4>(zr + 32 * s);
    }
    f32x16 oR, oI;
    dft576(z, C, oR, oI);
    const rsrc_t ro = __builtin_amdgcn_make_buffer_rsrc((void*)P.out, 0, 2048 * 2304, 0x00020000);
    if (a < 24) {
#pragma unroll
        for (int i = 0; i < 3; ++i) ldsw<u32x2>(ow + 16 * i, (u32x2){pk(oR[4 * i], oR[4 * i + 1]), pk(oR[4 * i + 2], oR[4 * i + 3])});
#pragma unroll
        for (int i = 0; i < 3; ++i) ldsw<u32x2>(ow + 1152 + 16 * i, (u32x2){pk(oI[4 * i], oI[4 * i + 1]), pk(oI[4 * i + 2], oI[4 * i + 3])});
    }
    u32x4 o0 = ldsr<u32x4>(orr), o1 = ldsr<u32x4>(orr + 1152), o2 = ldsr<u32x4>(ot);
    __builtin_amdgcn_sched_barrier(0);
    __builtin_amdgcn_raw_buffer_store_b128(o0, ro, oo, 0, SC1);
    __builtin_amdgcn_raw_buffer_store_b128(o1, ro, oo, 64 * 2304, SC1);
    PIN(o2);
    if (l < 16) __builtin_amdgcn_raw_buffer_store_b128(o2, ro, oot, 0, SC1);
}

DI int rowperm(int c) { return (c & 8) + 2 * (c & 3) + ((c >> 2) & 1); }
DI void u_issue(const Params& P, int cg, int lb, int w, int l) {
    const u32x4 us = make_srd(P.u, 2048 * 1152);
    const unsigned vo = rowperm(l & 15) * 1152 + (l >> 4) * 16, so = (unsigned)(32 * lb + 16 * (w & 1)) * 1152 + cg * 384 + 96 * (w & 2);
    dma16(us, vo, so, A_STG + 3072 * w); dma16(us, vo, so + 64, A_STG + 3072 * w + 1024); dma16(us, vo, so + 128, A_STG + 3072 * w + 2048);
}
DI void phase_t1(const Ctx& X, int cg, int lb, int w, int l, LAS char* lds, u32x4 (&ur)[3], u32x4 (&dv)[3]) {
    const int c = l & 15, g = l >> 4, q = c >> 2, pp = c & 3;
    LAS char* rp = lds + A_STG + ((g >> 1) + (w & 2)) * 3072 + (6 * (w & 1) + (pp >> 1)) * 256 + (8 * (g & 1) + 4 * (q & 1) + (q >> 1)) * 16 + 8 * (pp & 1);
    unsigned so = TOFF + (unsigned)(192 * cg + 48 * w + c) * TROW + (unsigned)(32 * lb + 8 * g) * 2;
    PIN(rp); PIN(so);
    BAR();
#pragma unroll
    for (int it = 0; it < 3; ++it) {
        const u32x2 lo = trread(rp + 512 * it), hi = trread(rp + 512 * it + 32);
        __builtin_amdgcn_raw_buffer_store_b128((u32x4){lo[0], lo[1], hi[0], hi[1]}, ws_rsrc(X), so, it * 16 * TROW, SC1);
    }
    LAS char* up = lds + A_STG + 3072 * w + l * 16; LAS char* dp = lds + A_PAR + 1024 * w + 832 + 16 * g;
#pragma unroll
    for (int m = 0; m < 3; ++m) { ur[m] = ldsr<u32x4>(up + 1024 * m); dv[m] = ldsr<u32x4>(dp + 64 * m); }
#pragma unroll
    for (int m = 0; m < 3; ++m) { PIN(ur[m]); PIN(dv[m]); }
}
DI f32x2 zpow(float al2, float brev, float p) { const float e = __builtin_amdgcn_exp2f(al2 * p), t = __builtin_amdgcn_fractf(brev * p); return (f32x2){e * cosr(t), e * sinr(t)}; }
struct Modes { unsigned la, aim, cr, ci, ldt; };
DI Modes read_modes(LAS char* par, int ch, int l) {
    return {ldsr<u16>(par + 2 * l), ldsr<u16>(par + 128 + 2 * l), ldsr<u16>(par + 256 + 2 * l), ldsr<u16>(par + 384 + 2 * l), ldsr<u16>(par + 512 + 2 * (ch & 1))};
}
template <int MID, class F> DI void kgen_tables(const Modes& M, LAS char* tab, int l, F mid) {
    const float dt = bfround(__builtin_amdgcn_exp2f(bflo(M.ldt) * 1.44269504088896f));
    const float al2 = -bfround(__builtin_amdgcn_exp2f(bflo(M.la) * 1.44269504088896f)) * dt * 1.44269504088896f, brev = bflo(M.aim) * dt * 0.15915494309189535f;
    const f32x2 z8 = zpow(al2, brev, 8.f), z32 = zpow(al2, brev, 32.f), y1 = zpow(al2, -brev, 1.f), y256 = zpow(al2, -brev, 256.f);
    LAS char* Lp = tab + l * 4; LAS char* Hp = tab + 32 * 272 + l * 4;
    f32x2 L[4]; L[0] = (f32x2){1.f, 0.f}; L[1] = z8; L[2] = cmul(z8, z8); L[3] = cmul(L[2], z8);
    f32x2 H[8]; H[0] = (f32x2){bflo(M.cr), -bflo(M.ci)} * (2.f * dt);
#pragma unroll
    for (int jj = 0; jj < 8; ++jj) {
#pragma unroll
        for (int k = 0; k < 4; ++k) { ldsw<unsigned>(Lp + (4 * jj + k) * 272, pk2(L[k])); if (jj < 7) L[k] = cmul(L[k], z32); }
        ldsw<unsigned>(Hp + jj * 272, pk2(H[jj])); if (jj < 7) H[jj + 1] = cmul(H[jj], y1);
        if (jj == MID) { __builtin_amdgcn_sched_barrier(0); mid(); __builtin_amdgcn_sched_barrier(0); }
    }
#pragma unroll
    for (int k = 0; k < 4; ++k) { H[2 * k] = cmul(H[2 * k], y256); ldsw<unsigned>(Hp + (8 + k) * 272, pk2(H[2 * k])); }
#pragma unroll
    for (int k = 0; k < 4; ++k) { const int s = 4 * (k & 1); H[s] = cmul(H[s], y256); ldsw<unsigned>(Hp + (12 + k) * 272, pk2(H[s])); }
#pragma unroll
    for (int k = 0; k < 5; ++k) { H[0] = cmul(H[0], y256); ldsw<unsigned>(Hp + (16 + k) * 272, pk2(H[0])); }
}
DI void kgen_half(const Modes& M, LAS char* tab, bool offs, int l) {
    const float dt = bfround(__builtin_amdgcn_exp2f(bflo(M.ldt) * 1.44269504088896f));
    const float al2 = -bfround(__builtin_amdgcn_exp2f(bflo(M.la) * 1.44269504088896f)) * dt * 1.44269504088896f, brev = bflo(M.aim) * dt * 0.15915494309189535f;
    if (!offs) {
        const f32x2 z8 = zpow(al2, brev, 8.f), z32 = zpow(al2, brev, 32.f);
        LAS char* Lp = tab + l * 4;
        f32x2 L[4]; L[0] = (f32x2){1.f, 0.f}; L[1] = z8; L[2] = cmul(z8, z8); L[3] = cmul(L[2], z8);
#pragma unroll
        for (int jj = 0; jj < 8; ++jj)
#pragma unroll
            for (int k = 0; k < 4; ++k) { ldsw<unsigned>(Lp + (4 * jj + k) * 272, pk2(L[k])); if (jj < 7) L[k] = cmul(L[k], z32); }
    } else {
        const f32x2 y1 = zpow(al2, -brev, 1.f), y256 = zpow(al2, -brev, 256.f);
        LAS char* Hp = tab + 32 * 272 + l * 4;
        f32x2 H[8]; H[0] = (f32x2){bflo(M.cr), -bflo(M.ci)} * (2.f * dt);
#pragma unroll
        for (int jj = 0; jj < 8; ++jj) { ldsw<unsigned>(Hp + jj * 272, pk2(H[jj])); if (jj < 7) H[jj + 1] = cmul(H[jj], y1); }
#pragma unroll
        for (int k = 0; k < 4; ++k) { H[2 * k] = cmul(H[2 * k], y256); ldsw<unsigned>(Hp + (8 + k) * 272, pk2(H[2 * k])); }
#pragma unroll
        for (int k = 0; k < 4; ++k) { const int s = 4 * (k & 1); H[s] = cmul(H[s], y256); ldsw<unsigned>(Hp + (12 + k) * 272, pk2(H[s])); }
#pragma unroll
        for (int k = 0; k < 5; ++k) { H[0] = cmul(H[0], y256); ldsw<unsigned>(Hp + (16 + k) * 272, pk2(H[0])); }
    }
}
DI f32x16 kgen_contract(LAS char* tab, int l) {
    const int jc = l & 31, h = l >> 5;
    LAS char* ap = tab + (32 + (jc < 21 ? jc : 20)) * 272 + 16 * h; LAS char* bp = tab + jc * 272 + 16 * h;
    u32x4 A[8], B[8];
#pragma unroll
    for (int s = 0; s < 8; ++s) { A[s] = ldsr<u32x4>(ap + 32 * s); B[s] = ldsr<u32x4>(bp + 32 * s); }
    __builtin_amdgcn_sched_barrier(0);
    f32x16 d = {};
#pragma unroll
    for (int s = 0; s < 8; ++s) d = mfma32(A[s], B[s], d);
    return d;
}
DI void kgen_store(const f32x16& d, LAS char* kp, int l) {
    const int jc = l & 31, h = l >> 5;
    ldsw<f32x4>(kp + (8 * jc + 4 * h) * 4, (f32x4){d[0], d[1], d[2], d[3]});
    ldsw<f32x2>(kp + (h ? 384 + 2 * jc : 256 + 4 * jc) * 4, (f32x2){d[4], d[5]});
    ldsw<f32x2>(kp + (h ? 448 + 2 * jc : 258 + 4 * jc) * 4, (f32x2){d[6], d[7]});
#pragma unroll
    for (int i = 0; i < 4; ++i) ldsw<float>(kp + (512 + 128 * h + 32 * i + jc) * 4, d[8 + i]);
}
DI void kstore(LAS char* dst, u32x4 v, unsigned prev) {
    ldsw<u32x4>(dst + C_K1, v);
    ldsw<u32x4>(dst + C_K0, (u32x4){__builtin_amdgcn_alignbit(v[1], v[0], 16), __builtin_amdgcn_alignbit(v[2], v[1], 16), __builtin_amdgcn_alignbit(v[3], v[2], 16), __builtin_amdgcn_alignbit(prev, v[3], 16)});
}
DI void kexpand_near(LAS char* k0, LAS char* ch, int hi, int l) {
    LAS char* k1 = k0 + 3072;
    if (hi == 0) {
        const bool s0 = l < 32;
        const int base = (s0 ? 8 * l : 128 + 4 * l) * 4, pb = l ? base - 4 : 0;
        const f32x4 a = ldsr<f32x4>(k0 + base) + ldsr<f32x4>(k1 + base), b = ldsr<f32x4>(k0 + base + 16) + ldsr<f32x4>(k1 + base + 16);
        const float p = ldsr<float>(k0 + pb) + ldsr<float>(k1 + pb);
        kstore(ch + (2040 - 8 * l) * 2, (u32x4){pk(s0 ? b[3] : b[0], s0 ? b[2] : a[3]), pk(s0 ? b[1] : a[2], s0 ? b[0] : a[2]), pk(s0 ? a[3] : a[2], s0 ? a[2] : a[1]), pk(s0 ? a[1] : a[0], a[0])},
               pk(l == 0 ? 0.f : l <= 32 ? p : a[0], 0.f));
    } else {
        const int base = (384 + 2 * l) * 4;
        const f32x2 a = ldsr<f32x2>(k0 + base) + ldsr<f32x2>(k1 + base); const float b = ldsr<float>(k0 + base + 8) + ldsr<float>(k1 + base + 8);
        const unsigned prev = pk(a[0], a[0]);
        kstore(ch + (1528 - 8 * l) * 2, (u32x4){pk(b, b), pk(a[1], a[1]), pk(a[1], a[0]), prev}, prev);
    }
}
DI void kexpand_far(LAS char* k0, LAS char* ch, int hi, int l) {
    LAS char* p = k0 + (512 + 64 * hi + l) * 4;
    const float v0 = ldsr<float>(p) + ldsr<float>(p + 3072), v1 = ldsr<float>(p + 4) + ldsr<float>(p + 3076);
    const unsigned lo = pk(v0, v0), up = pk(v1, v1), mid = pk(v1, (l & 1) ? v1 : v0);
    LAS char* dst = ch + (1016 - 512 * hi - 8 * l) * 2;
    ldsw<u32x4>(dst + C_K1, (u32x4){up, mid, lo, lo});
    ldsw<u32x4>(dst + C_K0, (u32x4){up, __builtin_amdgcn_alignbit(lo, mid, 16), lo, lo});
}
struct Frags { u32x4 F[8], G[8]; };
DI void frag_load(Frags& S, int i, LAS char* fp, LAS char* gp) {
    const u32x2 a = *(volatile LAS u32x2*)(fp + 512 * i), b = *(volatile LAS u32x2*)(fp + 512 * i + 8);
    S.F[i] = (u32x4){a[0], a[1], b[0], b[1]};
    S.G[i] = *(volatile LAS u32x4*)(gp + 512 * (7 - i));
}
DI void conv_step(f32x4 (&acc)[8], const Frags& C, Frags& N, LAS char* fp, LAS char* gp) {
#pragma unroll
    for (int i = 0; i < 8; ++i) {
        frag_load(N, i, fp, gp);
#pragma unroll
        for (int t = i; t < 8; ++t) acc[t] = mfma16(C.F[i], C.G[t - i], acc[t]);
    }
#pragma unroll
    for (int k = 0; k < 24; ++k) { __builtin_amdgcn_sched_group_barrier(0x008, 1, 0); __builtin_amdgcn_sched_group_barrier(0x100, 1, 0); }
    __builtin_amdgcn_sched_group_barrier(0x008, 12, 0);
}
DI void store_y(const Ctx& X, unsigned off, int t, f32x4 v) {
    __builtin_amdgcn_raw_buffer_store_b64((u32x2){pk(v[0], v[1]), pk(v[2], v[3])}, ws_rsrc(X), off, 512 * t, SC1);
}
struct ConvAddr { LAS char *fp, *gp, *dp; unsigned yo; };
template <bool TWO> DI void conv_prep(const Ctx& X, ConvAddr& A, int ch0, unsigned waitflag, unsigned utflag, bool tile, int w, int l, LAS char* lds) {
    LAS char* kp = lds + (TWO ? A_STG : 2 * TABB);
    if (TWO && tile) {
        DRAIN(); BAR();
        publish8(X, utflag, 192 * FSTR);
    }
    f32x16 kd16;
    if (TWO || w < 2) kd16 = kgen_contract(lds + w * TABB, l);
    __builtin_amdgcn_sched_barrier(0);
    const int kch = TWO ? (w >> 1) : 0, res0 = TWO ? 4 * (w & 1) : 2 * w;
    const int r = l & 15, g = l >> 4;
    A.fp = lds + kch * CHB + ((r & 1) ? 52 : C_U2 + 48) + 4 * r - 16 * g + 64 * res0;
    A.gp = lds + kch * CHB + ((r & 1) ? C_K1 : C_K0) + 496 - 64 * (r >> 1) - 16 * g + 64 * res0;
    A.yo = TOFF + (unsigned)(ch0 + kch) * TROW + (unsigned)(64 * (r >> 1) + 16 * g + 8 * (r & 1));
    A.dp = lds + (TWO ? 2 : 1) * CHB + l * 16;
    LAS char* zp = lds + (l < 32 ? kch * CHB + ((w & 1) ? C_K1 : C_K0) + 4096 + 16 * l : ((l >> 3) & 1) * CHB + ((l & 4) ? C_U2 : 0) + 16 * (l & 3));
    const bool zon = l < 32 ? (l < 29 && (TWO || w < 2)) : (w == 3 && l < (TWO ? 48 : 40));
    unsigned l16 = l * 16;
    PIN(A.fp); PIN(A.gp); PIN(A.yo); PIN(A.dp); PIN(zp); PIN(l16);
    const u32x4 srd = ws_srd(X);
    __builtin_amdgcn_sched_barrier(0);
    if (TWO || w < 2) kgen_store(kd16, kp + w * 3072, l);
    if (!TWO && w == 3) { __builtin_amdgcn_s_sleep(DL_UTX); wait_flags(X, waitflag + l * FSTR); }
    BAR();
    if (zon) ldsw<u32x4>(zp, (u32x4){0, 0, 0, 0});
    if (!TWO && w == 3) {
        asm volatile("s_waitcnt lgkmcnt(0)" ::: "memory");
        dma16x4_sc1(srd, l16, TOFF + (unsigned)ch0 * TROW, 64); dma16x4_sc1(srd, l16, TOFF + (unsigned)ch0 * TROW, C_U2 + 60);
    }
    if (TWO) {
        kexpand_far(kp + (kch ^ 1) * 6144, lds + (kch ^ 1) * CHB, w & 1, l);
        kexpand_near(kp + kch * 6144, lds + kch * CHB, w & 1, l);
    } else if (w < 2) kexpand_near(kp, lds, w, l);
    else kexpand_far(kp, lds, w & 1, l);
    if (TWO && w == 3) {
        if (tile) __builtin_amdgcn_s_sleep(DL_UT); else __builtin_amdgcn_s_sleep(DL_UTN);
        wait_flags(X, waitflag + l * FSTR);
        asm volatile("s_waitcnt lgkmcnt(0)" ::: "memory");
        const unsigned uo = TOFF + (unsigned)ch0 * TROW;
        dma16x4_sc1(srd, l16, uo, 64); dma16x4_sc1(srd, l16, uo, C_U2 + 60);
        dma16x4_sc1(srd, l16, uo + TROW, CHB + 64); dma16x4_sc1(srd, l16, uo + TROW, CHB + C_U2 + 60);
    }
    asm volatile("s_waitcnt vmcnt(0) lgkmcnt(0)\n\ts_barrier" ::: "memory");
}
DI void conv_main(f32x4 (&acc)[8], LAS char* fp, LAS char* gp, int nit) {
#pragma unroll
    for (int t = 0; t < 8; ++t) acc[t] = (f32x4){};
    Frags S0, S1;
    frag_load(S0, 0, fp, gp);
#pragma unroll
    for (int i = 1; i < 8; ++i) S0.G[i] = *(volatile LAS u32x4*)(gp + 512 * (7 - i));
#pragma unroll
    for (int i = 1; i < 8; ++i) { const u32x2 a = *(volatile LAS u32x2*)(fp + 512 * i), b = *(volatile LAS u32x2*)(fp + 512 * i + 8); S0.F[i] = (u32x4){a[0], a[1], b[0], b[1]}; }
#pragma unroll 1
    for (int it = 0; it < nit; ++it) {
        conv_step(acc, S0, S1, fp + 64, gp + 64);
        conv_step(acc, S1, S0, fp + 128, gp + 128);
        fp += 128; gp += 128;
    }
}
template <bool TWO> DI void conv_finish(const Ctx& X, const ConvAddr& A, const f32x4 (&acc)[8], int w) {
    if (TWO) {
        LAS char* mp = A.dp + w * 4096; LAS char* op = A.dp + (w ^ 1) * 4096;
        if (w & 1) {
#pragma unroll
            for (int t = 0; t < 4; ++t) ldsw<f32x4>(mp + t * 1024, acc[t]);
            BAR();
#pragma unroll
            for (int t = 0; t < 4; ++t) store_y(X, A.yo, 4 + t, add4(acc[4 + t], ldsr<f32x4>(op + t * 1024)));
        } else {
#pragma unroll
            for (int t = 0; t < 4; ++t) ldsw<f32x4>(mp + t * 1024, acc[4 + t]);
            BAR();
#pragma unroll
            for (int t = 0; t < 4; ++t) store_y(X, A.yo, t, add4(acc[t], ldsr<f32x4>(op + t * 1024)));
        }
    } else {
#pragma unroll
        for (int t = 0; t < 8; ++t) ldsw<f32x4>(A.dp + (w * 8 + t) * 1024, acc[t]);
        BAR();
        LAS char* op = A.dp + w * 2048;
#pragma unroll
        for (int k = 0; k < 2; ++k)
            store_y(X, A.yo + 1024 * w, k, add4(add4(ldsr<f32x4>(op + k * 1024), ldsr<f32x4>(op + (8 + k) * 1024)), add4(ldsr<f32x4>(op + (16 + k) * 1024), ldsr<f32x4>(op + (24 + k) * 1024))));
    }
}
DI void phase_t2(const Params& P, const Ctx& X, unsigned pubflag, int rep, int cg, int lb, int w, int l, LAS char* lds, u32x4 (&ur)[3], const u32x4 (&dv)[3]) {
    const int c = l & 15, g = l >> 4, q = c >> 2, pp = c & 3;
#pragma unroll
    for (int m = 0; m < 3; ++m) {
#pragma unroll
        for (int i = 0; i < 4; ++i) ur[m][i] = pk(bflo(ur[m][i]) * bflo(dv[m][i]), bfhi(ur[m][i]) * bfhi(dv[m][i]));
        PIN(ur[m]);
    }
    unsigned go = (unsigned)(l >> 2) * TROW + (unsigned)(((l & 3) ^ ((l >> 4) & 2)) * 16);
    const unsigned gs = TOFF + (unsigned)(192 * cg + 48 * w) * TROW + 64 * lb;
    LAS char* rp = lds + (96 * (w >> 1) + 8 * g + q) * 64 + (((2 * (w & 1) + (pp >> 1)) ^ (2 * (g & 1))) * 16) + 8 * (pp & 1);
    const u32x4 srd = ws_srd(X);
    unsigned oo = (unsigned)(32 * lb + 16 * (w & 1) + rowperm(c)) * 2304 + cg * 384 + 192 * (w >> 1) + 16 * g;
    PIN(go); PIN(rp); PIN(oo);
    DRAIN(); BAR();
    publish8(X, pubflag, 320 * FSTR);
    if (w == 0) {
        if (cg < 2) __builtin_amdgcn_s_sleep(DL_YT); else __builtin_amdgcn_s_sleep(DL_YTX);
        const unsigned f = F_YT + (unsigned)(rep * 320 + 96 * cg + l) * FSTR;
        wait_flags2(X, f, f + (cg < 2 ? 32 : 64) * FSTR);
    }
    BAR();
    dma16_sc1(srd, go, gs, 3072 * w); dma16_sc1(srd, go, gs + 16 * TROW, 3072 * w + 1024); dma16_sc1(srd, go, gs + 32 * TROW, 3072 * w + 2048);
    asm volatile("s_waitcnt vmcnt(0)\n\ts_barrier" ::: "memory");
    const rsrc_t ro = __builtin_amdgcn_make_buffer_rsrc((void*)P.out, 0, 2048 * 2304, 0x00020000);
#pragma unroll
    for (int m = 0; m < 3; ++m) {
        const u32x2 lo = trread(rp + 2048 * m), hi = trread(rp + 2048 * m + 256);
        const u32x4 y = {lo[0], lo[1], hi[0], hi[1]};
        u32x4 r;
#pragma unroll
        for (int i = 0; i < 4; ++i) r[i] = pk(bflo(y[i]) + bflo(ur[m][i]), bfhi(y[i]) + bfhi(ur[m][i]));
        __builtin_amdgcn_raw_buffer_store_b128(r, ro, oo + 64 * m, 0, OAUX);
    }
}

__global__ void __launch_bounds__(256, 2) fused_kernel(Params P) {
    __shared__ __attribute__((aligned(16))) char smem[LDS_BYTES];
    LAS char* lds = (LAS char*)smem;
    const int b = blockIdx.x ^ 256, l = threadIdx.x & 63, w = rfl(threadIdx.x >> 6);
    u64 pc; unsigned rest;
    asm volatile("s_getpc_b64 %0\n.Lhere%=:\n\ts_mov_b32 %1, .Lkernel_tail-.Lhere%=+1024" : "=s"(pc), "=s"(rest));
    touch(make_srd((const void*)pc, rest), l << 20, ((blockIdx.x >> 3) * 4 + w) * 128, b < 256 ? L_JUNK : A_JUNK);
    asm volatile("" :: "s"(P.ws), "s"(P.x));
    __builtin_amdgcn_sched_barrier(0);
    const int s2 = (b & 7) * 8 + ((b >> 3) & 7), j = (b >> 6) & 3, rep = b & 7;
    const int k3 = (((b >> 3) & 31) * 11) >> 5, tcg = ((b >> 3) & 31) - 3 * k3, tlb = 2 * rep + (k3 & 1) + 8 * (k3 & ~1);
    const int ch0 = b < 256 ? 320 + b : 2 * b - 512;
    const int epo = (b < 256 ? B_PAR : A_PAR) + 1024 * w;
    u32x4 ur[3], dv[3];
    if (b < 256) p1_issue(P, s2, w, l);
    if (b >= 192) modes_issue(P, b < 256 ? ch0 : ch0 + (w >> 1), w & 1, l, epo);
    misc_issue(P, 192 * tcg + 96 * (w >> 1), l, epo);
    int bb = b; asm volatile("" : "+s"(bb) :: "memory");
    const bool roleB = bb < 256, extra = roleB && bb >= 192, tile = bb < 448;
    LAS char* par = lds + epo;
    Ctx X; X.ws = (GAS char*)P.ws;
    ConvAddr CA;
    if (roleB) {
        PRIO(3);
        phase_p1(X, par, s2, j, w, l, lds);
        const unsigned myflag = F_E1 + (unsigned)((4 * j + w) * 64 + s2) * FSTR;
        if (!extra) {
            phase_p2(P, X, p2_geom(bb), myflag, w, l, lds);
        } else {
            DRAIN(); if (l == 0) publish(X, myflag);
            kgen_half(read_modes(par, ch0, l), lds + (w & 1) * TABB, w >= 2, l);
            BAR();
            conv_prep<false>(X, CA, ch0, F_UT + (unsigned)(rep * 192 + 128) * FSTR, 0, false, w, l, lds);
        }
    } else {
        PRIO(1);
        DRAIN();
        if (tile) u_issue(P, tcg, tlb, w, l);
        X.tag = read_tag(par);
        kgen_tables<TMID>(read_modes(par, ch0 + (w >> 1), l), lds + w * TABB, l, [&]() __attribute__((always_inline)) {
            if (tile) { DRAIN(); phase_t1(X, tcg, tlb, w, l, lds, ur, dv); }
        });
        conv_prep<true>(X, CA, ch0, F_UT + (unsigned)(rep * 192 + (bb < 352 ? 0 : tile ? 64 : 128)) * FSTR, F_UT + (unsigned)(tcg * 64 + tlb) * FSTR, tile, w, l, lds);
    }
    f32x4 acc[8];
    if (!roleB || extra) conv_main(acc, CA.fp, CA.gp, roleB ? 1 : 2);
    if (roleB) {
        if (extra) conv_finish<false>(X, CA, acc, w);
        phase_p3(P, X, extra ? F_YT + (unsigned)(64 + bb) * FSTR : F_E2 + (unsigned)bb * FSTR, extra ? 320 * FSTR : 192 * FSTR, s2, j, w, l, lds);
    } else {
        conv_finish<true>(X, CA, acc, w);
        const unsigned yflag = F_YT + (unsigned)(bb - 256) * FSTR;
        if (tile) phase_t2(P, X, yflag, rep, tcg, tlb, w, l, lds, ur, dv);
        else {
            DRAIN(); BAR();
            publish8(X, yflag, 320 * FSTR);
            if (RARE(bb == 511 && w == 0)) {
                wait_flags2(X, F_E1 + (unsigned)l * FSTR, F_E1 + (unsigned)(256 + l) * FSTR);
                wait_flags2(X, F_E1 + (unsigned)(512 + l) * FSTR, F_E1 + (unsigned)(768 + l) * FSTR);
                __builtin_amdgcn_s_sleep(DL_YT);
                const unsigned f = F_YT + (unsigned)(7 * 320 + l) * FSTR;
                wait_flags2(X, f, f + 64 * FSTR); wait_flags2(X, f + 128 * FSTR, f + 192 * FSTR);
                if (l == 0) __hip_atomic_store((GAS u64*)(P.ws + EPOCH_OFF), X.tag, RLX_AGENT);
            }
        }
    }
    asm volatile(".Lkernel_tail:");
}

extern "C" void kernel_launch(void* const* d_in, const int* in_sizes, int n_in, void* d_out, int out_size, void* d_ws, size_t ws_size, hipStream_t stream) {
    Params P;
    P.u = (const u16*)d_in[0]; P.logA = (const u16*)d_in[1]; P.Aim = (const u16*)d_in[2]; P.Cre = (const u16*)d_in[3]; P.Cim = (const u16*)d_in[4];
    P.logdt = (const u16*)d_in[5]; P.Dskip = (const u16*)d_in[6]; P.x = (const u16*)d_in[7]; P.wr = (const u16*)d_in[8]; P.wi = (const u16*)d_in[9];
    P.out = (u16*)d_out; P.ws = (char*)d_ws;
    fused_kernel<<<512, 256, 0, stream>>>(P);
}
```
